```python
import torch

def _softmax(x, axis=-1):
    return torch.softmax(x, dim=axis)
def _gelu(x):
    return torch.nn.functional.gelu(x, approximate="tanh")
def _silu(x):
    return torch.nn.functional.silu(x)
def _sigmoid(x):
    return torch.sigmoid(x)
def _layernorm(x, axis=-1, eps=1e-5):
    m = x.mean(dim=axis, keepdim=True)
    v = x.var(dim=axis, keepdim=True, unbiased=False)
    return (x - m) / torch.sqrt(v + eps)
def _scatter_add(tgt, idx, src, axis=0):
    return tgt.clone().index_add_(axis, idx, src)
def _segment_sum(src, seg, n):
    out = torch.zeros((n,) + src.shape[1:], dtype=src.dtype)
    return out.index_add_(0, seg, src)
def _pad(x, pw):
    flat = tuple(v for pair in reversed(pw) for v in pair)
    return torch.nn.functional.pad(x, flat)

kernel_name = 'recurrent_scan__sliding_block__glu_gated_swiglu__prompt'

def setup_inputs():

    _w = (lambda a: (lambda t: t.to(torch.bfloat16) if t.is_floating_point() else t)(torch.as_tensor(a)))
    def _A_setup():
        B, S, D = 1, 2048, 512
        return {
            "x": _w((lambda *s: torch.randn(*s, dtype=torch.bfloat16) * 0.05)(B, S, D)),
            "wqkv": _w((lambda *s: torch.randn(*s, dtype=torch.bfloat16) * 0.2)(D, 3 * D)),
            "wo": _w((lambda *s: torch.randn(*s, dtype=torch.bfloat16) * 0.2)(D, D)),
        }
    _A_in = _A_setup()

    def _B_setup():
        B, S, D, F = 1, 2048, 512, 1376
        return {
            "x": _w((lambda *s: torch.randn(*s, dtype=torch.bfloat16) * 0.05)(B, S, D)),
            "w1": _w((lambda *s: torch.randn(*s, dtype=torch.bfloat16) * 0.1)(D, F) * (2.0 / D) ** 0.5),
            "w3": _w((lambda *s: torch.randn(*s, dtype=torch.bfloat16) * 0.1)(D, F) * (2.0 / D) ** 0.5),
            "w2": _w((lambda *s: torch.randn(*s, dtype=torch.bfloat16) * 0.1)(F, D) * (2.0 / F) ** 0.5),
        }
    _B_in = _B_setup()

    return {
        "A_x": _A_in["x"],
        "A_wqkv": _A_in["wqkv"],
        "A_wo": _A_in["wo"],
        "B_x": _B_in["x"],
        "B_w1": _B_in["w1"],
        "B_w3": _B_in["w3"],
        "B_w2": _B_in["w2"],
    }

def reference(A_x, A_wqkv, A_wo, B_x, B_w1, B_w3, B_w2):
    def _A_ref(x, wqkv, wo):
        B, S, D = x.shape
        H = 32; Dh = D // H; W = 256
        qkv = (lambda x, *d: x.permute(*d))((x @ wqkv).reshape(B, S, 3, H, Dh), 2, 0, 3, 1, 4)
        q, k, v = qkv[0], qkv[1], qkv[2]
        scores = torch.einsum("bhqd,bhkd->bhqk", q, k) * (Dh ** -0.5)
        idx = torch.arange(S)
        mask = (idx[:, None] - idx[None, :] >= 0) & (idx[:, None] - idx[None, :] < W)
        scores = torch.where(mask[None, None], scores, -1e9)
        p = _softmax(scores, axis=-1)
        o = torch.einsum("bhqk,bhkd->bhqd", p, v)
        return (lambda x, *d: x.permute(*d))(o, 0, 2, 1, 3).reshape(B, S, D) @ wo

    def _B_ref(x, w1, w3, w2):
        gate = torch.einsum("bsd,df->bsf", x, w1)
        up = torch.einsum("bsd,df->bsf", x, w3)
        h = _silu(gate) * up
        out = torch.einsum("bsf,fd->bsd", h, w2)
        return out

    x = A_x
    xn = _layernorm(x, axis=-1)
    s = _A_ref(xn, A_wqkv, A_wo)
    g = _silu(_B_ref(xn, B_w1, B_w3, B_w2))
    return x + s * g
```

```cpp
#include <hip/hip_runtime.h>
#include <hip/hip_bf16.h>
#include <stdint.h>

typedef unsigned short u16;
__device__ __forceinline__ float bf2f(u16 v) { return __uint_as_float(((unsigned)v) << 16); }
__device__ __forceinline__ u16 f2bf(float f) { unsigned u = __float_as_uint(f); u += 0x7fffu + ((u >> 16) & 1u); return (u16)(u >> 16); }
__device__ __forceinline__ float rbf(float f) { return bf2f(f2bf(f)); }

constexpr int S = 2048, D = 512, F = 1376, H = 32, DH = 16, W = 256;

__global__ void ln_kernel(const u16* x, u16* xn) {
    int row = blockIdx.x * 4 + (threadIdx.x >> 6), lane = threadIdx.x & 63;
    const u16* xr = x + (size_t)row * D + lane * 8;
    float v[8]; float s = 0.f;
    for (int j = 0; j < 8; ++j) { v[j] = bf2f(xr[j]); s += v[j]; }
    for (int o = 1; o < 64; o <<= 1) s += __shfl_xor(s, o);
    float mean = s * (1.f / D);
    float q = 0.f;
    for (int j = 0; j < 8; ++j) { float d = v[j] - mean; q += d * d; }
    for (int o = 1; o < 64; o <<= 1) q += __shfl_xor(q, o);
    float var = q * (1.f / D);
    float mb = rbf(mean), vb = rbf(var);
    float den = rbf(sqrtf(rbf(vb + 1e-5f)));
    u16* o = xn + (size_t)row * D + lane * 8;
    for (int j = 0; j < 8; ++j) o[j] = f2bf(rbf(v[j] - mb) / den);
}

__global__ void gemm_naive(const u16* A, const u16* B, u16* C, int M, int N, int K) {
    int n = blockIdx.x * 64 + (threadIdx.x & 63);
    int m = blockIdx.y * 4 + (threadIdx.x >> 6);
    if (n >= N || m >= M) return;
    float acc = 0.f;
    const u16* a = A + (size_t)m * K;
    for (int k = 0; k < K; ++k) acc += bf2f(a[k]) * bf2f(B[(size_t)k * N + n]);
    C[(size_t)m * N + n] = f2bf(acc);
}

__global__ void swiglu_kernel(const u16* gate, const u16* up, u16* h, int n) {
    int i = blockIdx.x * 256 + threadIdx.x;
    if (i >= n) return;
    float g = bf2f(gate[i]);
    float sg = rbf(g / (1.f + __expf(-g)));
    h[i] = f2bf(sg * bf2f(up[i]));
}

__global__ void attn_naive(const u16* qkv, u16* o) {
    int idx = blockIdx.x * 256 + threadIdx.x;
    int row = idx >> 5, h = idx & 31;
    float q[DH];
    for (int d = 0; d < DH; ++d) q[d] = bf2f(qkv[(size_t)row * 1536 + h * DH + d]);
    int k0 = row - (W - 1); if (k0 < 0) k0 = 0;
    float mx = -1e30f;
    for (int k = k0; k <= row; ++k) {
        float s = 0.f;
        for (int d = 0; d < DH; ++d) s += q[d] * bf2f(qkv[(size_t)k * 1536 + 512 + h * DH + d]);
        s = rbf(s) * 0.25f;
        mx = fmaxf(mx, s);
    }
    float sum = 0.f;
    for (int k = k0; k <= row; ++k) {
        float s = 0.f;
        for (int d = 0; d < DH; ++d) s += q[d] * bf2f(qkv[(size_t)k * 1536 + 512 + h * DH + d]);
        s = rbf(s) * 0.25f;
        sum += __expf(s - mx);
    }
    float acc[DH];
    for (int d = 0; d < DH; ++d) acc[d] = 0.f;
    for (int k = k0; k <= row; ++k) {
        float s = 0.f;
        for (int d = 0; d < DH; ++d) s += q[d] * bf2f(qkv[(size_t)k * 1536 + 512 + h * DH + d]);
        s = rbf(s) * 0.25f;
        float p = rbf(__expf(s - mx) / sum);
        for (int d = 0; d < DH; ++d) acc[d] += p * bf2f(qkv[(size_t)k * 1536 + 1024 + h * DH + d]);
    }
    for (int d = 0; d < DH; ++d) o[(size_t)row * D + h * DH + d] = f2bf(acc[d]);
}

__global__ void final_kernel(const u16* x, const u16* s, const u16* b, u16* out, int n) {
    int i = blockIdx.x * 256 + threadIdx.x;
    if (i >= n) return;
    float bb = bf2f(b[i]);
    float g = rbf(bb / (1.f + __expf(-bb)));
    float sg = rbf(bf2f(s[i]) * g);
    out[i] = f2bf(bf2f(x[i]) + sg);
}

extern "C" void kernel_launch(void* const* d_in, const int* in_sizes, int n_in,
                              void* d_out, int out_size, void* d_ws, size_t ws_size,
                              hipStream_t stream) {
    const u16* x = (const u16*)d_in[0];
    const u16* wqkv = (const u16*)d_in[1];
    const u16* wo = (const u16*)d_in[2];
    const u16* w1 = (const u16*)d_in[4];
    const u16* w3 = (const u16*)d_in[5];
    const u16* w2 = (const u16*)d_in[6];
    u16* out = (u16*)d_out;
    char* ws = (char*)d_ws;
    u16* xn = (u16*)(ws);
    u16* qkv = (u16*)(ws + (4 << 20));
    u16* gate = (u16*)(ws + (16 << 20));
    u16* up = (u16*)(ws + (24 << 20));
    u16* h = (u16*)(ws + (32 << 20));
    u16* o = (u16*)(ws + (40 << 20));
    u16* s = (u16*)(ws + (44 << 20));
    u16* b = (u16*)(ws + (48 << 20));
    ln_kernel<<<S / 4, 256, 0, stream>>>(x, xn);
    gemm_naive<<<dim3(1536 / 64, S / 4), 256, 0, stream>>>(xn, wqkv, qkv, S, 1536, D);
    gemm_naive<<<dim3((F + 63) / 64, S / 4), 256, 0, stream>>>(xn, w1, gate, S, F, D);
    gemm_naive<<<dim3((F + 63) / 64, S / 4), 256, 0, stream>>>(xn, w3, up, S, F, D);
    swiglu_kernel<<<(S * F + 255) / 256, 256, 0, stream>>>(gate, up, h, S * F);
    attn_naive<<<S * H / 256, 256, 0, stream>>>(qkv, o);
    gemm_naive<<<dim3(D / 64, S / 4), 256, 0, stream>>>(o, wo, s, S, D, D);
    gemm_naive<<<dim3(D / 64, S / 4), 256, 0, stream>>>(h, w2, b, S, D, F);
    final_kernel<<<(S * D + 255) / 256, 256, 0, stream>>>(x, s, b, out, S * D);
}
```

```cpp
#include <hip/hip_runtime.h>
#include <stdint.h>

typedef unsigned short u16;
typedef __attribute__((ext_vector_type(8))) short bf16x8;
typedef __attribute__((ext_vector_type(4))) short s16x4;
typedef __attribute__((ext_vector_type(4))) float f32x4;
typedef __attribute__((ext_vector_type(2))) float f32x2;
typedef __attribute__((ext_vector_type(16))) float f32x16;
typedef __attribute__((ext_vector_type(4))) unsigned u32x4;
typedef __attribute__((ext_vector_type(2))) unsigned u32x2;
typedef __attribute__((ext_vector_type(2))) __bf16 bf16x2_t;
#define LAS __attribute__((address_space(3)))
#define DI __device__ __forceinline__

constexpr int S = 2048, D = 512, F = 1376, NQKV = 1536;
constexpr float LOG2E = 1.4426950408889634f;

DI float bflo(unsigned p) { return __uint_as_float(p << 16); }
DI float bfhi(unsigned p) { return __uint_as_float(p & 0xffff0000u); }
DI unsigned cvt_pk(float lo, float hi) { f32x2 v = {lo, hi}; bf16x2_t b = __builtin_convertvector(v, bf16x2_t); return __builtin_bit_cast(unsigned, b); }
DI float rbf(float v) { return bflo(cvt_pk(v, v)); }
DI float silu_f(float v) { return v * __builtin_amdgcn_rcpf(1.0f + __builtin_amdgcn_exp2f(-v * LOG2E)); }
DI s16x4 tr_read(const LAS char* p) { return __builtin_bit_cast(s16x4, __builtin_amdgcn_ds_read_tr16_b64_v4i16((LAS s16x4*)p)); }
DI bf16x8 lds_b128(const LAS char* p) { return *(const LAS bf16x8*)p; }
DI bf16x8 cat(s16x4 lo, s16x4 hi) { return __builtin_shufflevector(lo, hi, 0, 1, 2, 3, 4, 5, 6, 7); }
DI void glds16(const void* gsrc, unsigned lds_dst) {
    unsigned keep;
    asm volatile("s_mov_b32 %0, m0\n\ts_mov_b32 m0, %2\n\ts_nop 0\n\tglobal_load_lds_dwordx4 %1, off\n\ts_mov_b32 m0, %0" : "=&s"(keep) : "v"(gsrc), "s"(lds_dst) : "memory");
}
#define WAIT_BAR(N) asm volatile("s_waitcnt vmcnt(" #N ") lgkmcnt(0)\n\ts_barrier" ::: "memory")
#define MFMA16(a, b, c) __builtin_amdgcn_mfma_f32_16x16x32_bf16((a), (b), (c), 0, 0, 0)
#define MFMA32(a, b, c) __builtin_amdgcn_mfma_f32_32x32x16_bf16((a), (b), (c), 0, 0, 0)

__global__ __launch_bounds__(256) void k_ln(const u16* __restrict__ x, u16* __restrict__ xn) {
    const int row = blockIdx.x * 4 + (threadIdx.x >> 6), lane = threadIdx.x & 63;
    const u32x4 raw = *(const u32x4*)(x + (size_t)row * D + lane * 8);
    float v[8];
#pragma unroll
    for (int j = 0; j < 4; ++j) { v[2 * j] = bflo(raw[j]); v[2 * j + 1] = bfhi(raw[j]); }
    float s = 0.f;
#pragma unroll
    for (int j = 0; j < 8; ++j) s += v[j];
#pragma unroll
    for (int o = 1; o < 64; o <<= 1) s += __shfl_xor(s, o);
    const float mean = s * (1.f / D);
    float q = 0.f;
#pragma unroll
    for (int j = 0; j < 8; ++j) { const float d = v[j] - mean; q += d * d; }
#pragma unroll
    for (int o = 1; o < 64; o <<= 1) q += __shfl_xor(q, o);
    const float var = q * (1.f / D);
    const float mb = rbf(mean), vb = rbf(var);
    const float den = rbf(sqrtf(rbf(vb + 1e-5f)));
    u32x4 o;
#pragma unroll
    for (int j = 0; j < 4; ++j) o[j] = cvt_pk(rbf(v[2 * j] - mb) / den, rbf(v[2 * j + 1] - mb) / den);
    *(u32x4*)(xn + (size_t)row * D + lane * 8) = o;
}

constexpr int G1_SLOT = 36864, G1_NS = 4;
template <bool FFN>
DI void gemm1_body(char* smem, const u16* __restrict__ xn, const u16* __restrict__ wa, const u16* __restrict__ wb, const int ldw, const int c0, const int cmax,
                   u16* __restrict__ out, const int ldo, const int rb) {
    constexpr int NT = FFN ? 18 : 16;
    constexpr int NG = FFN ? 9 : 8;
    constexpr int PITCH = FFN ? 576 : 512;
    const int tid = threadIdx.x, l = tid & 63, w = __builtin_amdgcn_readfirstlane(tid >> 6);
    const int i = l & 15, g = l >> 4, q = i >> 2, p = l & 3;
    const unsigned lds0 = (unsigned)(uintptr_t)smem;
    const LAS char* lds = (const LAS char*)smem;
    bf16x8 xf[2][16];
    {
        const u16* xr = xn + (size_t)(rb * 128 + 32 * w + i) * D + 8 * g;
#pragma unroll
        for (int mt = 0; mt < 2; ++mt)
#pragma unroll
            for (int ks = 0; ks < 16; ++ks) xf[mt][ks] = *(const bf16x8*)(xr + mt * 16 * D + ks * 32);
    }
    const u16* gsrc[NG];
#pragma unroll
    for (int e = 0; e < NG; ++e) {
        const int c = (w * NG + e) * 64 + l;
        if (FFN) {
            const int r = c / 36, chp = c % 36, tp = chp >> 1, hf8 = chp & 1;
            const int t = tp ^ ((r >> 3) & 1);
            const bool up = t >= 9; const int tt = up ? t - 9 : t;
            int col = c0 + 16 * tt + 8 * hf8; col = col > cmax ? cmax : col;
            gsrc[e] = (up ? wb : wa) + (size_t)r * ldw + col;
        } else {
            const int r = c >> 5, chp = c & 31;
            const int sg = (r & 3) | (((r >> 3) & 1) << 2);
            const int ch = chp ^ (sg << 1);
            gsrc[e] = wa + (size_t)r * ldw + c0 + 8 * ch;
        }
    }
    const unsigned ldsw = lds0 + (unsigned)(w * NG) * 1024u;
    auto issue = [&](int st) {
        const unsigned dst = ldsw + (unsigned)(st % G1_NS) * G1_SLOT;
#pragma unroll
        for (int e = 0; e < NG; ++e) glds16(gsrc[e] + (size_t)st * 64 * ldw, dst + e * 1024u);
    };
    int ra[8];
    if (FFN) {
        const int base = (8 * g + q) * PITCH + 8 * p;
        ra[0] = base + (g & 1) * 32; ra[1] = base - (g & 1) * 32;
    } else {
        const int sg = q | ((g & 1) << 2);
#pragma unroll
        for (int t = 0; t < 8; ++t) ra[t] = (8 * g + q) * PITCH + ((t ^ sg) * 32) + 8 * p;
    }
    f32x4 acc[NT][2];
#pragma unroll
    for (int t = 0; t < NT; ++t) { acc[t][0] = (f32x4){0.f, 0.f, 0.f, 0.f}; acc[t][1] = (f32x4){0.f, 0.f, 0.f, 0.f}; }

    issue(0); issue(1); issue(2);
#pragma unroll
    for (int st = 0; st < 8; ++st) {
        if (st <= 5) { if (NG == 9) WAIT_BAR(18); else WAIT_BAR(16); }
        else if (st == 6) { if (NG == 9) WAIT_BAR(9); else WAIT_BAR(8); }
        else WAIT_BAR(0);
        if (st + 3 < 8) issue(st + 3);
        const LAS char* sb = lds + (st % G1_NS) * G1_SLOT;
#pragma unroll
        for (int s = 0; s < 2; ++s) {
#pragma unroll
            for (int t = 0; t < NT; ++t) {
                const LAS char* a = FFN ? sb + ra[t & 1] + t * 32 : sb + ra[t & 7] + (t >> 3) * 256;
                const s16x4 lo = tr_read(a + s * 32 * PITCH), hi = tr_read(a + s * 32 * PITCH + 4 * PITCH);
                const bf16x8 wf = cat(lo, hi);
                acc[t][0] = MFMA16(wf, xf[0][2 * st + s], acc[t][0]);
                acc[t][1] = MFMA16(wf, xf[1][2 * st + s], acc[t][1]);
            }
        }
    }
    const int row0 = rb * 128 + 32 * w + i;
    if (FFN) {
        unsigned hp[9][2][2];
#pragma unroll
        for (int t = 0; t < 9; ++t)
#pragma unroll
            for (int mt = 0; mt < 2; ++mt) {
                float hv[4];
#pragma unroll
                for (int r = 0; r < 4; r += 2) {
                    const unsigned gp = cvt_pk(acc[t][mt][r], acc[t][mt][r + 1]), upk = cvt_pk(acc[t + 9][mt][r], acc[t + 9][mt][r + 1]);
                    const unsigned sp = cvt_pk(silu_f(bflo(gp)), silu_f(bfhi(gp)));
                    hv[r] = bflo(sp) * bflo(upk); hv[r + 1] = bfhi(sp) * bfhi(upk);
                }
                hp[t][mt][0] = cvt_pk(hv[0], hv[1]); hp[t][mt][1] = cvt_pk(hv[2], hv[3]);
            }
#pragma unroll
        for (int mt = 0; mt < 2; ++mt) {
            u16* orow = out + (size_t)(row0 + 16 * mt) * ldo;
#pragma unroll
            for (int u = 0; u < 4; ++u) {
                auto r0 = __builtin_amdgcn_permlane16_swap(hp[2 * u][mt][0], hp[2 * u + 1][mt][0], false, false);
                auto r1 = __builtin_amdgcn_permlane16_swap(hp[2 * u][mt][1], hp[2 * u + 1][mt][1], false, false);
                const int col = c0 + 32 * u + 16 * (g & 1) + 8 * (g >> 1);
                if (col <= cmax) *(u32x4*)(orow + col) = (u32x4){r0[0], r1[0], r0[1], r1[1]};
            }
            const int col = c0 + 128 + 4 * g;
            if (col < cmax + 8) *(u32x2*)(orow + col) = (u32x2){hp[8][mt][0], hp[8][mt][1]};
        }
    } else {
#pragma unroll
        for (int mt = 0; mt < 2; ++mt) {
            u16* orow = out + (size_t)(row0 + 16 * mt) * ldo;
#pragma unroll
            for (int u = 0; u < 8; ++u) {
                const unsigned a0 = cvt_pk(acc[2 * u][mt][0], acc[2 * u][mt][1]), a1 = cvt_pk(acc[2 * u][mt][2], acc[2 * u][mt][3]);
                const unsigned b0 = cvt_pk(acc[2 * u + 1][mt][0], acc[2 * u + 1][mt][1]), b1 = cvt_pk(acc[2 * u + 1][mt][2], acc[2 * u + 1][mt][3]);
                auto r0 = __builtin_amdgcn_permlane16_swap(a0, b0, false, false);
                auto r1 = __builtin_amdgcn_permlane16_swap(a1, b1, false, false);
                const int col = c0 + 32 * u + 16 * (g & 1) + 8 * (g >> 1);
                *(u32x4*)(orow + col) = (u32x4){r0[0], r1[0], r0[1], r1[1]};
            }
        }
    }
}

__global__ __launch_bounds__(256, 1) void k_gemm1(const u16* __restrict__ xn, const u16* __restrict__ wqkv, const u16* __restrict__ w1, const u16* __restrict__ w3,
                                                  u16* __restrict__ qkv, u16* __restrict__ h) {
    __shared__ __attribute__((aligned(16))) char smem[G1_NS * G1_SLOT];
    const int b = blockIdx.x, xcd = b & 7, j = b >> 3;
    const int rb = 8 * (xcd & 1) + (j & 7), cg = 4 * (xcd >> 1) + (j >> 3);
    if (cg < 6) gemm1_body<false>(smem, xn, wqkv, wqkv, NQKV, cg * 256, NQKV - 8, qkv, NQKV, rb);
    else gemm1_body<true>(smem, xn, w1, w3, F, (cg - 6) * 144, F - 8, h, F, rb);
}

constexpr int AT_KOFF = 0, AT_VOFF = 40960, AT_ONES = 81920, AT_LDS = 81920 + 64;
__global__ __launch_bounds__(512, 2) void k_attn(const u16* __restrict__ qkv, u16* __restrict__ o) {
    __shared__ __attribute__((aligned(16))) char smem[AT_LDS];
    const int b = blockIdx.x, xcd = b & 7, j = b >> 3;
    const int qb = 4 * xcd + (j & 3), hg = j >> 2;
    const int q0 = qb * 64;
    const int tid = threadIdx.x, l = tid & 63, w = __builtin_amdgcn_readfirstlane(tid >> 6);
    const int hd = w & 3, qs = w >> 2;
    const unsigned lds0 = (unsigned)(uintptr_t)smem;
    const LAS char* lds = (const LAS char*)smem;
#pragma unroll
    for (int e = 0; e < 5; ++e) {
        const int piece = w * 5 + e, row = piece * 8 + (l >> 3), cp = l & 7;
        int key = q0 - 256 + row; key = key < 0 ? 0 : key;
        const int ck = cp ^ ((row >> 1) & 7), cv = cp ^ (((row >> 1) & 3) << 1);
        glds16(qkv + (size_t)key * NQKV + 512 + hg * 64 + 8 * ck, lds0 + AT_KOFF + piece * 1024);
        glds16(qkv + (size_t)key * NQKV + 1024 + hg * 64 + 8 * cv, lds0 + AT_VOFF + piece * 1024);
    }
    if (tid < 16) *(LAS unsigned*)((LAS char*)smem + AT_ONES + tid * 4) = 0x3f803f80u;
    const int c = l & 31, hh = l >> 5;
    const bf16x8 qf = *(const bf16x8*)(qkv + (size_t)(q0 + 32 * qs + c) * NQKV + (hg * 4 + hd) * 16 + 8 * hh);
    WAIT_BAR(0);
    const int kt0 = q0 + 32 * qs - 256;
    const int tmin = kt0 >= 0 ? 0 : (-kt0) >> 5;
    unsigned pk[9][8];
    float mx = -INFINITY;
    const LAS char* kaddr = lds + AT_KOFF + (32 * qs + c) * 128 + (((2 * hd + hh) ^ ((c >> 1) & 7)) * 16);
#pragma unroll
    for (int tt = 0; tt < 9; ++tt) {
        if (tt >= tmin) {
            const bf16x8 kf = lds_b128(kaddr + tt * 4096);
            f32x16 sc = {};
            sc = MFMA32(kf, qf, sc);
            if (tt == 0 || tt == 8) {
#pragma unroll
                for (int r = 0; r < 16; ++r) {
                    const int key = (r & 3) + 8 * (r >> 2) + 4 * hh;
                    const bool ok = tt == 0 ? key > c : key <= c;
                    sc[r] = ok ? sc[r] : -INFINITY;
                }
            }
#pragma unroll
            for (int r = 0; r < 16; ++r) mx = __builtin_fmaxf(mx, sc[r]);
#pragma unroll
            for (int e = 0; e < 8; ++e) pk[tt][e] = cvt_pk(sc[2 * e], sc[2 * e + 1]);
        }
    }
    { auto rr = __builtin_amdgcn_permlane32_swap(__float_as_uint(mx), __float_as_uint(mx), false, false); mx = __builtin_fmaxf(__uint_as_float(rr[0]), __uint_as_float(rr[1])); }
    constexpr float CS = 0.25f * LOG2E;
    const float nmc = -rbf(mx) * CS;
    f32x16 oa = {};
    const int i16 = l & 15, q4 = i16 >> 2, p4 = l & 3, onesl = (l >> 4) & 1;
#pragma unroll
    for (int tt = 0; tt < 9; ++tt) {
        if (tt >= tmin) {
            unsigned pp[8];
#pragma unroll
            for (int e = 0; e < 8; ++e) {
                const float p0 = __builtin_amdgcn_exp2f(__builtin_fmaf(bflo(pk[tt][e]), CS, nmc));
                const float p1 = __builtin_amdgcn_exp2f(__builtin_fmaf(bfhi(pk[tt][e]), CS, nmc));
                pp[e] = cvt_pk(p0, p1);
            }
#pragma unroll
            for (int s = 0; s < 2; ++s) {
                const bf16x8 pf = __builtin_bit_cast(bf16x8, (u32x4){pp[4 * s], pp[4 * s + 1], pp[4 * s + 2], pp[4 * s + 3]});
                const int r0 = 32 * qs + 32 * tt + 16 * s + 4 * hh + q4, r1 = r0 + 8;
                const int a0 = onesl ? AT_ONES + 8 * p4 : AT_VOFF + r0 * 128 + (((2 * hd) ^ (((r0 >> 1) & 3) << 1)) * 16) + 8 * p4;
                const int a1 = onesl ? AT_ONES + 8 * p4 : AT_VOFF + r1 * 128 + (((2 * hd) ^ (((r1 >> 1) & 3) << 1)) * 16) + 8 * p4;
                const bf16x8 vf = cat(tr_read(lds + a0), tr_read(lds + a1));
                oa = MFMA32(vf, pf, oa);
            }
        }
    }
    const float rl = 1.0f / oa[8];
    unsigned a0 = cvt_pk(oa[0] * rl, oa[1] * rl), a1 = cvt_pk(oa[2] * rl, oa[3] * rl);
    unsigned b0 = cvt_pk(oa[4] * rl, oa[5] * rl), b1 = cvt_pk(oa[6] * rl, oa[7] * rl);
    auto r0 = __builtin_amdgcn_permlane32_swap(a0, b0, false, false);
    auto r1 = __builtin_amdgcn_permlane32_swap(a1, b1, false, false);
    *(u32x4*)(o + (size_t)(q0 + 32 * qs + c) * D + (hg * 4 + hd) * 16 + 8 * hh) = (u32x4){r0[0], r1[0], r0[1], r1[1]};
}

constexpr int G2_NS = 8, G2_SLOT = 16384, G2_NST = 30;
__global__ __launch_bounds__(512, 2) void k_gemm2(const u16* __restrict__ o, const u16* __restrict__ h, const u16* __restrict__ wo, const u16* __restrict__ w2,
                                                  const u16* __restrict__ x, u16* __restrict__ out) {
    __shared__ __attribute__((aligned(16))) char smem[G2_NS * G2_SLOT];
    const int b = blockIdx.x, xcd = b & 7, j = b >> 3;
    const int rt = 4 * xcd + (j & 3), ct = j >> 2;
    const int row0 = rt * 64, col0 = ct * 64;
    const int tid = threadIdx.x, l = tid & 63, w = __builtin_amdgcn_readfirstlane(tid >> 6);
    const int kg = w >> 2, wm = (w >> 1) & 1, wn = w & 1;
    const int i = l & 15, g = l >> 4, q = i >> 2, p = l & 3;
    const unsigned lds0 = (unsigned)(uintptr_t)smem;
    const LAS char* lds = (const LAS char*)smem;
    const int prow = 8 * w + (l >> 3), cp = l & 7;
    const int ctok = cp ^ ((prow >> 1) & 7);
    const int sgw = ((prow >> 1) & 1) | (((prow >> 3) & 1) << 1);
    const int cw = cp ^ (sgw << 1);
    auto issue = [&](int st, int slot) {
        const unsigned dst = lds0 + (unsigned)slot * G2_SLOT + (unsigned)w * 1024u;
        if (st < 8) {
            glds16(o + (size_t)(row0 + prow) * D + st * 64 + 8 * ctok, dst);
            glds16(wo + (size_t)(st * 64 + prow) * D + col0 + 8 * cw, dst + 8192);
        } else {
            int k = (st - 8) * 64 + prow; k = k > F - 1 ? F - 1 : k;
            glds16(h + (size_t)(row0 + prow) * F + (st - 8) * 64 + 8 * ctok, dst);
            glds16(w2 + (size_t)k * D + col0 + 8 * cw, dst + 8192);
        }
    };
    int ta[2], wa[2];
#pragma unroll
    for (int mt = 0; mt < 2; ++mt) { const int r = 32 * wm + 16 * mt + i; ta[mt] = r * 128 + (((4 * kg + g) ^ ((r >> 1) & 7)) * 16); }
    {
        const int kr = 32 * kg + 8 * g + q;
        const int sg = ((kr >> 1) & 1) | (((kr >> 3) & 1) << 1);
#pragma unroll
        for (int nt = 0; nt < 2; ++nt) wa[nt] = 8192 + kr * 128 + (((2 * wn + nt) ^ sg) * 32) + 8 * p;
    }
    f32x4 accS[2][2], accB[2][2];
#pragma unroll
    for (int a = 0; a < 2; ++a)
#pragma unroll
        for (int c = 0; c < 2; ++c) { accS[a][c] = (f32x4){0.f, 0.f, 0.f, 0.f}; accB[a][c] = (f32x4){0.f, 0.f, 0.f, 0.f}; }
#pragma unroll
    for (int st = 0; st < G2_NS - 1; ++st) issue(st, st);
    for (int st = 0; st < G2_NST; ++st) {
        WAIT_BAR(12);
        { int sn = st + G2_NS - 1; const int slot = sn & (G2_NS - 1); sn = sn > G2_NST - 1 ? G2_NST - 1 : sn; issue(sn, slot); }
        const LAS char* sb = lds + (st & (G2_NS - 1)) * G2_SLOT;
        if (st == G2_NST - 1 && kg == 1) break;
        const bf16x8 t0 = lds_b128(sb + ta[0]), t1 = lds_b128(sb + ta[1]);
        const bf16x8 w0 = cat(tr_read(sb + wa[0]), tr_read(sb + wa[0] + 512)), w1f = cat(tr_read(sb + wa[1]), tr_read(sb + wa[1] + 512));
        if (st < 8) {
            accS[0][0] = MFMA16(w0, t0, accS[0][0]); accS[0][1] = MFMA16(w0, t1, accS[0][1]);
            accS[1][0] = MFMA16(w1f, t0, accS[1][0]); accS[1][1] = MFMA16(w1f, t1, accS[1][1]);
        } else {
            accB[0][0] = MFMA16(w0, t0, accB[0][0]); accB[0][1] = MFMA16(w0, t1, accB[0][1]);
            accB[1][0] = MFMA16(w1f, t0, accB[1][0]); accB[1][1] = MFMA16(w1f, t1, accB[1][1]);
        }
    }
    WAIT_BAR(0);
    {
        LAS f32x4* xb = (LAS f32x4*)smem + ((wm * 2 + wn) * 2 + (kg ^ 1)) * 4 * 64 + l;
        xb[0 * 64] = kg ? accS[0][0] : accS[0][1]; xb[1 * 64] = kg ? accS[1][0] : accS[1][1];
        xb[2 * 64] = kg ? accB[0][0] : accB[0][1]; xb[3 * 64] = kg ? accB[1][0] : accB[1][1];
    }
    __syncthreads();
    f32x4 sS[2], sB[2];
    {
        const LAS f32x4* xb = (const LAS f32x4*)smem + ((wm * 2 + wn) * 2 + kg) * 4 * 64 + l;
        sS[0] = (kg ? accS[0][1] : accS[0][0]) + xb[0 * 64]; sS[1] = (kg ? accS[1][1] : accS[1][0]) + xb[1 * 64];
        sB[0] = (kg ? accB[0][1] : accB[0][0]) + xb[2 * 64]; sB[1] = (kg ? accB[1][1] : accB[1][0]) + xb[3 * 64];
    }
    const int row = row0 + 32 * wm + 16 * kg + i;
#pragma unroll
    for (int nt = 0; nt < 2; ++nt) {
        const int col = col0 + 32 * wn + 16 * nt + 4 * g;
        const u32x2 xv = *(const u32x2*)(x + (size_t)row * D + col);
        float r[4];
#pragma unroll
        for (int e = 0; e < 4; e += 2) {
            const unsigned sp = cvt_pk(sS[nt][e], sS[nt][e + 1]), bp = cvt_pk(sB[nt][e], sB[nt][e + 1]);
            const unsigned gp = cvt_pk(silu_f(bflo(bp)), silu_f(bfhi(bp)));
            const unsigned mp = cvt_pk(bflo(sp) * bflo(gp), bfhi(sp) * bfhi(gp));
            const unsigned xw = xv[e >> 1];
            r[e] = bflo(xw) + bflo(mp); r[e + 1] = bfhi(xw) + bfhi(mp);
        }
        *(u32x2*)(out + (size_t)row * D + col) = (u32x2){cvt_pk(r[0], r[1]), cvt_pk(r[2], r[3])};
    }
}

extern "C" void kernel_launch(void* const* d_in, const int* in_sizes, int n_in, void* d_out, int out_size, void* d_ws, size_t ws_size, hipStream_t stream) {
    const u16* x = (const u16*)d_in[0];
    const u16* wqkv = (const u16*)d_in[1];
    const u16* wo = (const u16*)d_in[2];
    const u16* w1 = (const u16*)d_in[4];
    const u16* w3 = (const u16*)d_in[5];
    const u16* w2 = (const u16*)d_in[6];
    char* ws = (char*)d_ws;
    u16* xn = (u16*)(ws);
    u16* qkv = (u16*)(ws + (4 << 20));
    u16* h = (u16*)(ws + (12 << 20));
    u16* o = (u16*)(ws + (20 << 20));
    k_ln<<<S / 4, 256, 0, stream>>>(x, xn);
    k_gemm1<<<256, 256, 0, stream>>>(xn, wqkv, w1, w3, qkv, h);
    k_attn<<<256, 512, 0, stream>>>(qkv, o);
    k_gemm2<<<256, 512, 0, stream>>>(o, h, wo, w2, x, (u16*)d_out);
}
```

```cpp
#include <hip/hip_runtime.h>
#include <stdint.h>

typedef unsigned short u16;
typedef __attribute__((ext_vector_type(8))) short bf16x8;
typedef __attribute__((ext_vector_type(4))) short s16x4;
typedef __attribute__((ext_vector_type(4))) float f32x4;
typedef __attribute__((ext_vector_type(2))) float f32x2;
typedef __attribute__((ext_vector_type(16))) float f32x16;
typedef __attribute__((ext_vector_type(4))) unsigned u32x4;
typedef __attribute__((ext_vector_type(2))) unsigned u32x2;
typedef __attribute__((ext_vector_type(2))) __bf16 bf16x2_t;
#define LAS __attribute__((address_space(3)))
#define DI __device__ __forceinline__

constexpr int S = 2048, D = 512, F = 1376, NQKV = 1536;
constexpr float LOG2E = 1.4426950408889634f;

DI float bflo(unsigned p) { return __uint_as_float(p << 16); }
DI float bfhi(unsigned p) { return __uint_as_float(p & 0xffff0000u); }
DI unsigned cvt_pk(float lo, float hi) { f32x2 v = {lo, hi}; bf16x2_t b = __builtin_convertvector(v, bf16x2_t); return __builtin_bit_cast(unsigned, b); }
DI float rbf(float v) { return bflo(cvt_pk(v, v)); }
DI float silu_f(float v) { return v * __builtin_amdgcn_rcpf(1.0f + __builtin_amdgcn_exp2f(-v * LOG2E)); }
DI s16x4 tr_read(const LAS char* p) { return __builtin_bit_cast(s16x4, __builtin_amdgcn_ds_read_tr16_b64_v4i16((LAS s16x4*)p)); }
DI bf16x8 lds_b128(const LAS char* p) { return *(const LAS bf16x8*)p; }
DI bf16x8 cat(s16x4 lo, s16x4 hi) { return __builtin_shufflevector(lo, hi, 0, 1, 2, 3, 4, 5, 6, 7); }
DI void glds16(const void* sbase, unsigned voff, unsigned lds_dst) {
    unsigned keep;
    asm volatile("s_mov_b32 %0, m0\n\ts_mov_b32 m0, %3\n\ts_nop 0\n\tglobal_load_lds_dwordx4 %1, %2\n\ts_mov_b32 m0, %0" : "=&s"(keep) : "v"(voff), "s"(sbase), "s"(lds_dst) : "memory");
}
template <int N> DI void wait_bar() { asm volatile("s_waitcnt vmcnt(%0) lgkmcnt(0)\n\ts_barrier" ::"i"(N) : "memory"); }
#define MFMA16(a, b, c) __builtin_amdgcn_mfma_f32_16x16x32_bf16((a), (b), (c), 0, 0, 0)
#define MFMA32(a, b, c) __builtin_amdgcn_mfma_f32_32x32x16_bf16((a), (b), (c), 0, 0, 0)
template <int V> struct IC { static constexpr int value = V; };
template <int I, int N, class Fn> DI void static_for(Fn&& f) { if constexpr (I < N) { f(IC<I>{}); static_for<I + 1, N>(f); } }
constexpr int imin(int a, int b) { return a < b ? a : b; }

__global__ __launch_bounds__(256) void k_ln(const u16* __restrict__ x, u16* __restrict__ xn) {
    const int row = blockIdx.x * 4 + (threadIdx.x >> 6), lane = threadIdx.x & 63;
    const u32x4 raw = *(const u32x4*)(x + (size_t)row * D + lane * 8);
    float v[8];
#pragma unroll
    for (int j = 0; j < 4; ++j) { v[2 * j] = bflo(raw[j]); v[2 * j + 1] = bfhi(raw[j]); }
    float s = 0.f;
#pragma unroll
    for (int j = 0; j < 8; ++j) s += v[j];
#pragma unroll
    for (int o = 1; o < 64; o <<= 1) s += __shfl_xor(s, o);
    const float mean = s * (1.f / D);
    float q = 0.f;
#pragma unroll
    for (int j = 0; j < 8; ++j) { const float d = v[j] - mean; q += d * d; }
#pragma unroll
    for (int o = 1; o < 64; o <<= 1) q += __shfl_xor(q, o);
    const float var = q * (1.f / D);
    const float mb = rbf(mean), vb = rbf(var);
    const float den = rbf(sqrtf(rbf(vb + 1e-5f)));
    u32x4 o;
#pragma unroll
    for (int j = 0; j < 4; ++j) o[j] = cvt_pk(rbf(v[2 * j] - mb) / den, rbf(v[2 * j + 1] - mb) / den);
    *(u32x4*)(xn + (size_t)row * D + lane * 8) = o;
}

constexpr int G1_S = 5, G1_SLOT = 28672, G1_N = 16;
template <bool FFN>
DI void gemm1_body(char* smem, const u16* __restrict__ xn, const u16* __restrict__ wa, const u16* __restrict__ wb, const int ldw, const int c0, const int cmax,
                   u16* __restrict__ out, const int ldo, const int rb) {
    constexpr int NT = FFN ? 4 : 5;
    constexpr int PITCH = FFN ? 256 : 640;
    constexpr int NWL = FFN ? 4 : 5;
    constexpr int G = 2 + NWL;
    const int tid = threadIdx.x, l = tid & 63, w = __builtin_amdgcn_readfirstlane(tid >> 6);
    const unsigned lds0 = (unsigned)(uintptr_t)smem;
    const LAS char* lds = (const LAS char*)smem;
    if (w >= 4) {
        const int lw = w - 4;
        unsigned toff[2];
#pragma unroll
        for (int e = 0; e < 2; ++e) {
            const int row = 16 * (2 * lw + e) + (l >> 2);
            toff[e] = (unsigned)((rb * 128 + row) * D + 8 * ((l & 3) ^ ((row >> 2) & 3))) * 2u;
        }
        unsigned woff[NWL];
#pragma unroll
        for (int e = 0; e < NWL; ++e) {
            if (FFN) {
                const int cl = ((lw + 4 * e) & 7) * 64 + l, r = cl >> 4, c = (cl & 15) ^ ((r & 3) << 2);
                int col = c0 + 8 * c; col = col > cmax ? cmax : col;
                woff[e] = (unsigned)(r * ldw + col) * 2u;
            } else {
                const int cl = (lw + 4 * e) * 64 + l, r = cl / 40, c = (cl % 40) ^ (((r >> 1) & 1) << 2);
                int col = c0 + 8 * c; col = col > cmax ? cmax : col;
                woff[e] = (unsigned)(r * ldw + col) * 2u;
            }
        }
        auto issue = [&](int st) {
            const unsigned dst = lds0 + (unsigned)(st % G1_S) * G1_SLOT;
#pragma unroll
            for (int e = 0; e < 2; ++e) glds16(xn, toff[e] + st * 64, dst + (2 * lw + e) * 1024);
#pragma unroll
            for (int e = 0; e < NWL; ++e) glds16(FFN && e >= 2 ? wb : wa, woff[e] + (unsigned)(st * 32 * ldw) * 2u, dst + 8192 + (lw + 4 * e) * 1024);
        };
        issue(0); issue(1); issue(2); issue(3);
        static_for<0, G1_N>([&](auto ic) {
            constexpr int st = decltype(ic)::value;
            wait_bar<G * (imin(st - 2 + G1_S, G1_N - 1) - imin(st + 1, G1_N - 1))>();
            if constexpr (st - 1 + G1_S < G1_N) issue(st - 1 + G1_S);
        });
        return;
    }
    const int wm = w >> 1, wn = w & 1;
    const int c = l & 31, hh = l >> 5, i16 = l & 15, gb = (l >> 4) & 1, q = i16 >> 2, p = l & 3;
    int ta[2][2];
#pragma unroll
    for (int mt = 0; mt < 2; ++mt) { ta[mt][0] = (64 * wm + 32 * mt + c) * 64 + ((hh ^ ((c >> 2) & 3)) * 16); ta[mt][1] = ta[mt][0] ^ 32; }
    int ra[4];
    {
        const int base = 8192 + (8 * hh + q) * PITCH + gb * 32 + 8 * p;
        if (FFN) {
#pragma unroll
            for (int t = 0; t < 4; ++t) ra[t] = base + ((t ^ q) * 64);
        } else { const int b = (q >> 1) & 1; ra[0] = base + b * 64; ra[1] = base - b * 64; }
    }
    f32x16 acc[NT][2];
#pragma unroll
    for (int t = 0; t < NT; ++t) { acc[t][0] = (f32x16){}; acc[t][1] = (f32x16){}; }
    bf16x8 tf[2][2], wf[2][NT];
    auto read_frags = [&](int st, int s, int buf) {
        const LAS char* sb = lds + (st % G1_S) * G1_SLOT;
        tf[buf][0] = lds_b128(sb + ta[0][s]); tf[buf][1] = lds_b128(sb + ta[1][s]);
#pragma unroll
        for (int t = 0; t < NT; ++t) {
            const int T = FFN ? 2 * wn + (t & 1) : 5 * wn + t;
            const LAS char* a = FFN ? sb + ra[T & 3] + (t >> 1) * 8192 : sb + ra[T & 1] + T * 64;
            wf[buf][t] = cat(tr_read(a + s * 16 * PITCH), tr_read(a + s * 16 * PITCH + 4 * PITCH));
        }
    };
    auto mma = [&](int buf) {
#pragma unroll
        for (int t = 0; t < NT; ++t) { acc[t][0] = MFMA32(wf[buf][t], tf[buf][0], acc[t][0]); acc[t][1] = MFMA32(wf[buf][t], tf[buf][1], acc[t][1]); }
    };
    auto sched = [&]() {
#pragma unroll
        for (int t = 0; t < NT + 1; ++t) { __builtin_amdgcn_sched_group_barrier(0x100, 2, 0); __builtin_amdgcn_sched_group_barrier(0x008, 1, 0); }
        __builtin_amdgcn_sched_group_barrier(0x008, NT, 0);
    };
    static_for<0, G1_N>([&](auto ic) {
        constexpr int st = decltype(ic)::value;
        asm volatile("s_waitcnt lgkmcnt(0)\n\ts_barrier" ::: "memory");
        if constexpr (st == 0) read_frags(0, 0, 0);
        __builtin_amdgcn_sched_barrier(0);
        read_frags(st, 1, 1);
        mma(0);
        sched();
        __builtin_amdgcn_sched_barrier(0);
        if constexpr (st + 1 < G1_N) { read_frags(st + 1, 0, 0); mma(1); sched(); } else mma(1);
        __builtin_amdgcn_sched_barrier(0);
    });
    const int row0 = rb * 128 + 64 * wm + c;
    auto store_tile = [&](const float (&v)[16], u16* orow, int col, bool ok1, bool ok2) {
        unsigned a0 = cvt_pk(v[0], v[1]), a1 = cvt_pk(v[2], v[3]), b0 = cvt_pk(v[4], v[5]), b1 = cvt_pk(v[6], v[7]);
        unsigned c0_ = cvt_pk(v[8], v[9]), c1 = cvt_pk(v[10], v[11]), d0 = cvt_pk(v[12], v[13]), d1 = cvt_pk(v[14], v[15]);
        auto r0 = __builtin_amdgcn_permlane32_swap(a0, b0, false, false), r1 = __builtin_amdgcn_permlane32_swap(a1, b1, false, false);
        auto r2 = __builtin_amdgcn_permlane32_swap(c0_, d0, false, false), r3 = __builtin_amdgcn_permlane32_swap(c1, d1, false, false);
        if (ok1) *(u32x4*)(orow + col + 8 * hh) = (u32x4){r0[0], r1[0], r0[1], r1[1]};
        if (ok2) *(u32x4*)(orow + col + 16 + 8 * hh) = (u32x4){r2[0], r3[0], r2[1], r3[1]};
    };
#pragma unroll
    for (int mt = 0; mt < 2; ++mt) {
        u16* orow = out + (size_t)(row0 + 32 * mt) * ldo;
        if (FFN) {
#pragma unroll
            for (int t = 0; t < 2; ++t) {
                float v[16];
#pragma unroll
                for (int r = 0; r < 16; ++r) v[r] = silu_f(acc[t][mt][r]) * acc[t + 2][mt][r];
                const int col = c0 + 64 * wn + 32 * t;
                store_tile(v, orow, col, col + 8 * hh <= cmax, col + 16 + 8 * hh <= cmax);
            }
        } else {
#pragma unroll
            for (int t = 0; t < 5; ++t) {
                float v[16];
#pragma unroll
                for (int r = 0; r < 16; ++r) v[r] = acc[t][mt][r];
                const int col = c0 + 160 * wn + 32 * t;
                store_tile(v, orow, col, col + 8 * hh <= cmax, col + 16 + 8 * hh <= cmax);
            }
        }
    }
}

__global__ __launch_bounds__(512, 2) void k_gemm1(const u16* __restrict__ xn, const u16* __restrict__ wqkv, const u16* __restrict__ w1, const u16* __restrict__ w3,
                                                  u16* __restrict__ qkv, u16* __restrict__ h) {
    __shared__ __attribute__((aligned(16))) char smem[G1_S * G1_SLOT];
    const int b = blockIdx.x, xcd = b & 7, j = b >> 3;
    const int rb = 8 * (xcd & 1) + (j & 7), cg = 4 * (xcd >> 1) + (j >> 3);
    if (cg < 5) gemm1_body<false>(smem, xn, wqkv, wqkv, NQKV, cg * 320, NQKV - 8, qkv, NQKV, rb);
    else gemm1_body<true>(smem, xn, w1, w3, F, (cg - 5) * 128, F - 8, h, F, rb);
}

constexpr int AT_KOFF = 0, AT_VOFF = 40960, AT_ONES = 81920, AT_LDS = 81920 + 64;
__global__ __launch_bounds__(512, 2) void k_attn(const u16* __restrict__ qkv, u16* __restrict__ o) {
    __shared__ __attribute__((aligned(16))) char smem[AT_LDS];
    const int b = blockIdx.x, xcd = b & 7, j = b >> 3;
    const int qb = 4 * xcd + (j & 3), hg = j >> 2;
    const int q0 = qb * 64;
    const int tid = threadIdx.x, l = tid & 63, w = __builtin_amdgcn_readfirstlane(tid >> 6);
    const int hd = w & 3, qs = w >> 2;
    const unsigned lds0 = (unsigned)(uintptr_t)smem;
    const LAS char* lds = (const LAS char*)smem;
    const int c = l & 31, hh = l >> 5;
#pragma unroll
    for (int kv = 0; kv < 2; ++kv)
#pragma unroll
        for (int e = 0; e < 5; ++e) {
            const int piece = w * 5 + e, row = piece * 8 + (l >> 3), cp = l & 7;
            int key = q0 - 256 + row; key = key < 0 ? 0 : key;
            const int cs = kv == 0 ? cp ^ ((row >> 1) & 7) : cp ^ (((row >> 1) & 3) << 1);
            glds16(qkv, (unsigned)(key * NQKV + 512 * (kv + 1) + hg * 64 + 8 * cs) * 2u, lds0 + (kv ? AT_VOFF : AT_KOFF) + piece * 1024);
        }
    if (tid < 16) *(LAS unsigned*)((LAS char*)smem + AT_ONES + tid * 4) = 0x3f803f80u;
    bf16x8 qf = *(const bf16x8*)(qkv + (size_t)(q0 + 32 * qs + c) * NQKV + (hg * 4 + hd) * 16 + 8 * hh);
    asm volatile("" : "+v"(qf));
    wait_bar<0>();
    const int kt0 = q0 + 32 * qs - 256;
    const int tmin = kt0 >= 0 ? 0 : (-kt0) >> 5;
    unsigned pk[9][8];
    float mx = -INFINITY;
    const LAS char* kaddr = lds + AT_KOFF + (32 * qs + c) * 128 + (((2 * hd + hh) ^ ((c >> 1) & 7)) * 16);
#pragma unroll
    for (int tt = 0; tt < 9; ++tt) {
        if (tt >= tmin) {
            const bf16x8 kf = lds_b128(kaddr + tt * 4096);
            f32x16 sc = {};
            sc = MFMA32(kf, qf, sc);
            if (tt == 0 || tt == 8) {
#pragma unroll
                for (int r = 0; r < 16; ++r) {
                    const int key = (r & 3) + 8 * (r >> 2) + 4 * hh;
                    const bool ok = tt == 0 ? key > c : key <= c;
                    sc[r] = ok ? sc[r] : -INFINITY;
                }
            }
#pragma unroll
            for (int r = 0; r < 16; ++r) mx = __builtin_fmaxf(mx, sc[r]);
#pragma unroll
            for (int e = 0; e < 8; ++e) pk[tt][e] = cvt_pk(sc[2 * e], sc[2 * e + 1]);
        }
    }
    { auto rr = __builtin_amdgcn_permlane32_swap(__float_as_uint(mx), __float_as_uint(mx), false, false); mx = __builtin_fmaxf(__uint_as_float(rr[0]), __uint_as_float(rr[1])); }
    constexpr float CS = 0.25f * LOG2E;
    const float nmc = -rbf(mx) * CS;
    f32x16 oa = {};
    const int i16 = l & 15, q4 = i16 >> 2, p4 = l & 3, onesl = (l >> 4) & 1;
#pragma unroll
    for (int tt = 0; tt < 9; ++tt) {
        if (tt >= tmin) {
            unsigned pp[8];
#pragma unroll
            for (int e = 0; e < 8; ++e) {
                const float p0 = __builtin_amdgcn_exp2f(__builtin_fmaf(bflo(pk[tt][e]), CS, nmc));
                const float p1 = __builtin_amdgcn_exp2f(__builtin_fmaf(bfhi(pk[tt][e]), CS, nmc));
                pp[e] = cvt_pk(p0, p1);
            }
#pragma unroll
            for (int s = 0; s < 2; ++s) {
                const bf16x8 pf = __builtin_bit_cast(bf16x8, (u32x4){pp[4 * s], pp[4 * s + 1], pp[4 * s + 2], pp[4 * s + 3]});
                const int r0 = 32 * qs + 32 * tt + 16 * s + 4 * hh + q4, r1 = r0 + 8;
                const int a0 = onesl ? AT_ONES + 8 * p4 : AT_VOFF + r0 * 128 + (((2 * hd) ^ (((r0 >> 1) & 3) << 1)) * 16) + 8 * p4;
                const int a1 = onesl ? AT_ONES + 8 * p4 : AT_VOFF + r1 * 128 + (((2 * hd) ^ (((r1 >> 1) & 3) << 1)) * 16) + 8 * p4;
                const bf16x8 vf = cat(tr_read(lds + a0), tr_read(lds + a1));
                oa = MFMA32(vf, pf, oa);
            }
        }
    }
    const float rl = 1.0f / oa[8];
    unsigned a0 = cvt_pk(oa[0] * rl, oa[1] * rl), a1 = cvt_pk(oa[2] * rl, oa[3] * rl);
    unsigned b0 = cvt_pk(oa[4] * rl, oa[5] * rl), b1 = cvt_pk(oa[6] * rl, oa[7] * rl);
    auto r0 = __builtin_amdgcn_permlane32_swap(a0, b0, false, false);
    auto r1 = __builtin_amdgcn_permlane32_swap(a1, b1, false, false);
    *(u32x4*)(o + (size_t)(q0 + 32 * qs + c) * D + (hg * 4 + hd) * 16 + 8 * hh) = (u32x4){r0[0], r1[0], r0[1], r1[1]};
}

constexpr int G2_S = 4, G2_SLOT = 32768, G2_N = 15;
__global__ __launch_bounds__(512, 2) void k_gemm2(const u16* __restrict__ o, const u16* __restrict__ h, const u16* __restrict__ wo, const u16* __restrict__ w2,
                                                  const u16* __restrict__ x, u16* __restrict__ out) {
    __shared__ __attribute__((aligned(16))) char smem[G2_S * G2_SLOT];
    const int b = blockIdx.x, xcd = b & 7, j = b >> 3;
    const int rt = 4 * xcd + (j & 3), ct = j >> 2;
    const int row0 = rt * 64, col0 = ct * 64;
    const int tid = threadIdx.x, l = tid & 63, w = __builtin_amdgcn_readfirstlane(tid >> 6);
    const unsigned lds0 = (unsigned)(uintptr_t)smem;
    const LAS char* lds = (const LAS char*)smem;
    if (w >= 4) {
        const int lw = w - 4;
        unsigned too[4], toh[4], wof[4], wofl[4];
#pragma unroll
        for (int e = 0; e < 4; ++e) {
            const int pc = 4 * lw + e;
            const int r = 4 * pc + (l >> 4), ctk = (l & 15) ^ (r & 15);
            too[e] = (unsigned)((row0 + r) * D + 8 * ctk) * 2u;
            toh[e] = (unsigned)((row0 + r) * F + 8 * ctk) * 2u;
            const int kr = 8 * pc + (l >> 3), cw = (l & 7) ^ (((kr >> 1) & 1) << 2);
            wof[e] = (unsigned)(kr * D + col0 + 8 * cw) * 2u;
            const int krl = kr + 1280 > F - 1 ? F - 1 : kr + 1280;
            wofl[e] = (unsigned)(krl * D + col0 + 8 * cw) * 2u;
        }
        auto issue = [&](int st) {
            const unsigned dst = lds0 + (unsigned)(st % G2_S) * G2_SLOT + (unsigned)(4 * lw) * 1024u;
#pragma unroll
            for (int e = 0; e < 4; ++e) {
                if (st < 4) { glds16(o, too[e] + st * 256, dst + e * 1024); glds16(wo, wof[e] + st * 128 * D * 2, dst + 16384 + e * 1024); }
                else if (st < G2_N - 1) { glds16(h, toh[e] + (st - 4) * 256, dst + e * 1024); glds16(w2, wof[e] + (st - 4) * 128 * D * 2, dst + 16384 + e * 1024); }
                else { glds16(h, toh[e] + (st - 4) * 256, dst + e * 1024); glds16(w2, wofl[e], dst + 16384 + e * 1024); }
            }
        };
        issue(0); issue(1); issue(2);
        wait_bar<16>();
        issue(3);
        static_for<0, G2_N>([&](auto ic) {
            constexpr int st = decltype(ic)::value;
            wait_bar<8 * (imin(st + G2_S - 1, G2_N - 1) - imin(st + 1, G2_N - 1))>();
            if constexpr (st + G2_S < G2_N) issue(st + G2_S);
        });
        return;
    }
    const int wm = w >> 1, wn = w & 1;
    const int c = l & 31, hh = l >> 5, i16 = l & 15, gb = (l >> 4) & 1, q = i16 >> 2, p = l & 3;
    const int trow = (32 * wm + c) * 256, tsw = c & 15;
    const int wa = 16384 + (8 * hh + q) * 128 + ((wn ^ ((q >> 1) & 1)) * 64) + gb * 32 + 8 * p;
    f32x16 accS = {}, accB = {};
    bf16x8 tf[2][8], wf[2][8];
    auto read_frags = [&](int st, int buf) {
        const LAS char* sb = lds + (st % G2_S) * G2_SLOT;
#pragma unroll
        for (int s = 0; s < 8; ++s) {
            tf[buf][s] = lds_b128(sb + trow + (((2 * s + hh) ^ tsw) * 16));
            wf[buf][s] = cat(tr_read(sb + wa + s * 2048), tr_read(sb + wa + s * 2048 + 512));
        }
    };
    asm volatile("s_waitcnt lgkmcnt(0)\n\ts_barrier" ::: "memory");
    read_frags(0, 0);
    static_for<0, G2_N>([&](auto ic) {
        constexpr int st = decltype(ic)::value, buf = st & 1, NS = st == G2_N - 1 ? 6 : 8;
        asm volatile("s_waitcnt lgkmcnt(0)\n\ts_barrier" ::: "memory");
        __builtin_amdgcn_sched_barrier(0);
        if constexpr (st + 1 < G2_N) read_frags(st + 1, buf ^ 1);
        __builtin_amdgcn_sched_barrier(0);
#pragma unroll
        for (int s = 0; s < NS; ++s) { if constexpr (st < 4) accS = MFMA32(wf[buf][s], tf[buf][s], accS); else accB = MFMA32(wf[buf][s], tf[buf][s], accB); }
        __builtin_amdgcn_sched_barrier(0);
    });
    const int row = row0 + 32 * wm + c, colb = col0 + 32 * wn;
    float v[16];
#pragma unroll
    for (int u = 0; u < 4; ++u) {
        const u32x2 xv = *(const u32x2*)(x + (size_t)row * D + colb + 8 * u + 4 * hh);
#pragma unroll
        for (int e = 0; e < 4; e += 2) {
            const unsigned sp = cvt_pk(accS[4 * u + e], accS[4 * u + e + 1]), bp = cvt_pk(accB[4 * u + e], accB[4 * u + e + 1]);
            const unsigned gp = cvt_pk(silu_f(bflo(bp)), silu_f(bfhi(bp)));
            const unsigned mp = cvt_pk(bflo(sp) * bflo(gp), bfhi(sp) * bfhi(gp));
            const unsigned xw = xv[e >> 1];
            v[4 * u + e] = bflo(xw) + bflo(mp); v[4 * u + e + 1] = bfhi(xw) + bfhi(mp);
        }
    }
    unsigned a0 = cvt_pk(v[0], v[1]), a1 = cvt_pk(v[2], v[3]), b0 = cvt_pk(v[4], v[5]), b1 = cvt_pk(v[6], v[7]);
    unsigned c0_ = cvt_pk(v[8], v[9]), c1 = cvt_pk(v[10], v[11]), d0 = cvt_pk(v[12], v[13]), d1 = cvt_pk(v[14], v[15]);
    auto r0 = __builtin_amdgcn_permlane32_swap(a0, b0, false, false), r1 = __builtin_amdgcn_permlane32_swap(a1, b1, false, false);
    auto r2 = __builtin_amdgcn_permlane32_swap(c0_, d0, false, false), r3 = __builtin_amdgcn_permlane32_swap(c1, d1, false, false);
    u16* orow = out + (size_t)row * D + colb + 8 * hh;
    *(u32x4*)(orow) = (u32x4){r0[0], r1[0], r0[1], r1[1]};
    *(u32x4*)(orow + 16) = (u32x4){r2[0], r3[0], r2[1], r3[1]};
}

extern "C" void kernel_launch(void* const* d_in, const int* in_sizes, int n_in, void* d_out, int out_size, void* d_ws, size_t ws_size, hipStream_t stream) {
    const u16* x = (const u16*)d_in[0];
    const u16* wqkv = (const u16*)d_in[1];
    const u16* wo = (const u16*)d_in[2];
    const u16* w1 = (const u16*)d_in[4];
    const u16* w3 = (const u16*)d_in[5];
    const u16* w2 = (const u16*)d_in[6];
    char* ws = (char*)d_ws;
    u16* xn = (u16*)(ws);
    u16* qkv = (u16*)(ws + (4 << 20));
    u16* h = (u16*)(ws + (12 << 20));
    u16* o = (u16*)(ws + (20 << 20));
    k_ln<<<S / 4, 256, 0, stream>>>(x, xn);
    k_gemm1<<<256, 512, 0, stream>>>(xn, wqkv, w1, w3, qkv, h);
    k_attn<<<256, 512, 0, stream>>>(qkv, o);
    k_gemm2<<<256, 512, 0, stream>>>(o, h, wo, w2, x, (u16*)d_out);
}
```

```cpp
#include <hip/hip_runtime.h>
#include <stdint.h>

typedef unsigned short u16;
typedef __attribute__((ext_vector_type(8))) short bf16x8;
typedef __attribute__((ext_vector_type(4))) short s16x4;
typedef __attribute__((ext_vector_type(4))) float f32x4;
typedef __attribute__((ext_vector_type(2))) float f32x2;
typedef __attribute__((ext_vector_type(16))) float f32x16;
typedef __attribute__((ext_vector_type(4))) unsigned u32x4;
typedef __attribute__((ext_vector_type(2))) unsigned u32x2;
typedef __attribute__((ext_vector_type(2))) __bf16 bf16x2_t;
#define LAS __attribute__((address_space(3)))
#define DI __device__ __forceinline__

constexpr int S = 2048, D = 512, F = 1376, NQKV = 1536;
constexpr float LOG2E = 1.4426950408889634f;

DI float bflo(unsigned p) { return __uint_as_float(p << 16); }
DI float bfhi(unsigned p) { return __uint_as_float(p & 0xffff0000u); }
DI unsigned cvt_pk(float lo, float hi) { f32x2 v = {lo, hi}; bf16x2_t b = __builtin_convertvector(v, bf16x2_t); return __builtin_bit_cast(unsigned, b); }
DI float rbf(float v) { return bflo(cvt_pk(v, v)); }
DI float silu_f(float v) { return v * __builtin_amdgcn_rcpf(1.0f + __builtin_amdgcn_exp2f(-v * LOG2E)); }
DI s16x4 tr_read(const LAS char* p) { return __builtin_bit_cast(s16x4, __builtin_amdgcn_ds_read_tr16_b64_v4i16((LAS s16x4*)p)); }
DI bf16x8 lds_b128(const LAS char* p) { return *(const LAS bf16x8*)p; }
DI bf16x8 cat(s16x4 lo, s16x4 hi) { return __builtin_shufflevector(lo, hi, 0, 1, 2, 3, 4, 5, 6, 7); }
DI void glds16(const void* sbase, unsigned voff, unsigned lds_dst) {
    unsigned keep;
    asm volatile("s_mov_b32 %0, m0\n\ts_mov_b32 m0, %3\n\ts_nop 0\n\tglobal_load_lds_dwordx4 %1, %2\n\ts_mov_b32 m0, %0" : "=&s"(keep) : "v"(voff), "s"(sbase), "s"(lds_dst) : "memory");
}
template <int N> DI void wait_bar() { asm volatile("s_waitcnt vmcnt(%0) lgkmcnt(0)\n\ts_barrier" ::"i"(N) : "memory"); }
#define MFMA16(a, b, c) __builtin_amdgcn_mfma_f32_16x16x32_bf16((a), (b), (c), 0, 0, 0)
#define MFMA32(a, b, c) __builtin_amdgcn_mfma_f32_32x32x16_bf16((a), (b), (c), 0, 0, 0)
template <int V> struct IC { static constexpr int value = V; };
template <int I, int N, class Fn> DI void static_for(Fn&& f) { if constexpr (I < N) { f(IC<I>{}); static_for<I + 1, N>(f); } }
constexpr int imin(int a, int b) { return a < b ? a : b; }

__global__ __launch_bounds__(256) void k_ln(const u16* __restrict__ x, u16* __restrict__ xn, const u16* __restrict__ wqkv, const u16* __restrict__ w1, const u16* __restrict__ w3,
                                              const u16* __restrict__ wo, const u16* __restrict__ w2) {
    unsigned pfv = 0;
    {
        const int gw = blockIdx.x * 4 + (threadIdx.x >> 6), ln = threadIdx.x & 63;
        int idx = gw * 25 + ln;
        if (ln < 25 && idx < 49408) {
            const u16* p;
            if (idx < 12288) p = wqkv + idx * 64;
            else if (idx < 23296) p = w1 + (idx - 12288) * 64;
            else if (idx < 34304) p = w3 + (idx - 23296) * 64;
            else if (idx < 38400) p = wo + (idx - 34304) * 64;
            else p = w2 + (idx - 38400) * 64;
            pfv = *(const unsigned*)p;
        }
    }
    const int row = blockIdx.x * 4 + (threadIdx.x >> 6), lane = threadIdx.x & 63;
    const u32x4 raw = *(const u32x4*)(x + (size_t)row * D + lane * 8);
    float v[8];
#pragma unroll
    for (int j = 0; j < 4; ++j) { v[2 * j] = bflo(raw[j]); v[2 * j + 1] = bfhi(raw[j]); }
    float s = 0.f;
#pragma unroll
    for (int j = 0; j < 8; ++j) s += v[j];
#pragma unroll
    for (int o = 1; o < 64; o <<= 1) s += __shfl_xor(s, o);
    const float mean = s * (1.f / D);
    float q = 0.f;
#pragma unroll
    for (int j = 0; j < 8; ++j) { const float d = v[j] - mean; q += d * d; }
#pragma unroll
    for (int o = 1; o < 64; o <<= 1) q += __shfl_xor(q, o);
    const float var = q * (1.f / D);
    const float mb = rbf(mean), vb = rbf(var);
    const float den = rbf(sqrtf(rbf(vb + 1e-5f)));
    u32x4 o;
#pragma unroll
    for (int j = 0; j < 4; ++j) o[j] = cvt_pk(rbf(v[2 * j] - mb) / den, rbf(v[2 * j + 1] - mb) / den);
    *(u32x4*)(xn + (size_t)row * D + lane * 8) = o;
    asm volatile("" :: "v"(pfv));
}

constexpr int G1_S = 5, G1_SLOT = 28672, G1_N = 16;
template <bool FFN>
DI void gemm1_body(char* smem, const u16* __restrict__ xn, const u16* __restrict__ wa, const u16* __restrict__ wb, const int ldw, const int c0, const int cmax,
                   u16* __restrict__ out, const int ldo, const int rb) {
    constexpr int NT = FFN ? 4 : 5;
    constexpr int PITCH = FFN ? 256 : 640;
    constexpr int NWL = FFN ? 4 : 5;
    constexpr int G = 2 + NWL;
    constexpr int EP = FFN ? 272 : 656;
    const int tid = threadIdx.x, l = tid & 63, w = __builtin_amdgcn_readfirstlane(tid >> 6);
    const unsigned lds0 = (unsigned)(uintptr_t)smem;
    const LAS char* lds = (const LAS char*)smem;
    if (w >= 4) {
        const int lw = w - 4;
        unsigned toff[2];
#pragma unroll
        for (int e = 0; e < 2; ++e) {
            const int row = 16 * (2 * lw + e) + (l >> 2);
            toff[e] = (unsigned)((rb * 128 + row) * D + 8 * ((l & 3) ^ ((row >> 2) & 3))) * 2u;
        }
        unsigned woff[NWL];
#pragma unroll
        for (int e = 0; e < NWL; ++e) {
            if (FFN) {
                const int cl = ((lw + 4 * e) & 7) * 64 + l, r = cl >> 4, c = (cl & 15) ^ ((r & 3) << 2);
                int col = c0 + 8 * c; col = col > cmax ? cmax : col;
                woff[e] = (unsigned)(r * ldw + col) * 2u;
            } else {
                const int cl = (lw + 4 * e) * 64 + l, r = cl / 40, c = (cl % 40) ^ (((r >> 1) & 1) << 2);
                int col = c0 + 8 * c; col = col > cmax ? cmax : col;
                woff[e] = (unsigned)(r * ldw + col) * 2u;
            }
        }
        auto issue = [&](int st) {
            const unsigned dst = lds0 + (unsigned)(st % G1_S) * G1_SLOT;
#pragma unroll
            for (int e = 0; e < 2; ++e) glds16(xn, toff[e] + st * 64, dst + (2 * lw + e) * 1024);
#pragma unroll
            for (int e = 0; e < NWL; ++e) glds16(FFN && e >= 2 ? wb : wa, woff[e] + (unsigned)(st * 32 * ldw) * 2u, dst + 8192 + (lw + 4 * e) * 1024);
        };
        issue(0); issue(1); issue(2); issue(3);
        static_for<0, G1_N>([&](auto ic) {
            constexpr int st = decltype(ic)::value;
            wait_bar<G * (imin(st - 2 + G1_S, G1_N - 1) - imin(st + 1, G1_N - 1))>();
            if constexpr (st - 1 + G1_S < G1_N) issue(st - 1 + G1_S);
        });
    } else {
    const int wm = w >> 1, wn = w & 1;
    const int c = l & 31, hh = l >> 5, i16 = l & 15, gb = (l >> 4) & 1, q = i16 >> 2, p = l & 3;
    int ta[2][2];
#pragma unroll
    for (int mt = 0; mt < 2; ++mt) { ta[mt][0] = (64 * wm + 32 * mt + c) * 64 + ((hh ^ ((c >> 2) & 3)) * 16); ta[mt][1] = ta[mt][0] ^ 32; }
    int ra[4];
    {
        const int base = 8192 + (8 * hh + q) * PITCH + gb * 32 + 8 * p;
        if (FFN) {
#pragma unroll
            for (int t = 0; t < 4; ++t) ra[t] = base + ((t ^ q) * 64);
        } else { const int b = (q >> 1) & 1; ra[0] = base + b * 64; ra[1] = base - b * 64; }
    }
    f32x16 acc[NT][2];
#pragma unroll
    for (int t = 0; t < NT; ++t) { acc[t][0] = (f32x16){}; acc[t][1] = (f32x16){}; }
    bf16x8 tf[2][2], wf[2][NT];
    auto read_frags = [&](int st, int s, int buf) {
        const LAS char* sb = lds + (st % G1_S) * G1_SLOT;
        tf[buf][0] = lds_b128(sb + ta[0][s]); tf[buf][1] = lds_b128(sb + ta[1][s]);
#pragma unroll
        for (int t = 0; t < NT; ++t) {
            const int T = FFN ? 2 * wn + (t & 1) : 5 * wn + t;
            const LAS char* a = FFN ? sb + ra[T & 3] + (t >> 1) * 8192 : sb + ra[T & 1] + T * 64;
            wf[buf][t] = cat(tr_read(a + s * 16 * PITCH), tr_read(a + s * 16 * PITCH + 4 * PITCH));
        }
    };
    auto mma = [&](int buf) {
#pragma unroll
        for (int t = 0; t < NT; ++t) { acc[t][0] = MFMA32(wf[buf][t], tf[buf][0], acc[t][0]); acc[t][1] = MFMA32(wf[buf][t], tf[buf][1], acc[t][1]); }
    };
    auto sched = [&]() {
#pragma unroll
        for (int t = 0; t < NT + 1; ++t) { __builtin_amdgcn_sched_group_barrier(0x100, 2, 0); __builtin_amdgcn_sched_group_barrier(0x008, 1, 0); }
        __builtin_amdgcn_sched_group_barrier(0x008, NT, 0);
    };
    static_for<0, G1_N>([&](auto ic) {
        constexpr int st = decltype(ic)::value;
        asm volatile("s_waitcnt lgkmcnt(0)\n\ts_barrier" ::: "memory");
        if constexpr (st == 0) read_frags(0, 0, 0);
        __builtin_amdgcn_sched_barrier(0);
        read_frags(st, 1, 1);
        mma(0);
        sched();
        __builtin_amdgcn_sched_barrier(0);
        if constexpr (st + 1 < G1_N) { read_frags(st + 1, 0, 0); mma(1); sched(); } else mma(1);
        __builtin_amdgcn_sched_barrier(0);
    });
    LAS char* stg = (LAS char*)smem + G1_SLOT;
    auto stage_tile = [&](const float (&v)[16], int row, int colb) {
        unsigned a0 = cvt_pk(v[0], v[1]), a1 = cvt_pk(v[2], v[3]), b0 = cvt_pk(v[4], v[5]), b1 = cvt_pk(v[6], v[7]);
        unsigned c0_ = cvt_pk(v[8], v[9]), c1 = cvt_pk(v[10], v[11]), d0 = cvt_pk(v[12], v[13]), d1 = cvt_pk(v[14], v[15]);
        auto r0 = __builtin_amdgcn_permlane32_swap(a0, b0, false, false), r1 = __builtin_amdgcn_permlane32_swap(a1, b1, false, false);
        auto r2 = __builtin_amdgcn_permlane32_swap(c0_, d0, false, false), r3 = __builtin_amdgcn_permlane32_swap(c1, d1, false, false);
        LAS char* p_ = stg + row * EP + colb + 16 * hh;
        *(LAS u32x4*)(p_) = (u32x4){r0[0], r1[0], r0[1], r1[1]};
        *(LAS u32x4*)(p_ + 32) = (u32x4){r2[0], r3[0], r2[1], r3[1]};
    };
#pragma unroll
    for (int mt = 0; mt < 2; ++mt) {
        const int row = 64 * wm + 32 * mt + c;
        if (FFN) {
#pragma unroll
            for (int t = 0; t < 2; ++t) {
                float v[16];
#pragma unroll
                for (int r = 0; r < 16; ++r) v[r] = silu_f(acc[t][mt][r]) * acc[t + 2][mt][r];
                stage_tile(v, row, 128 * wn + 64 * t);
            }
        } else {
#pragma unroll
            for (int t = 0; t < 5; ++t) {
                float v[16];
#pragma unroll
                for (int r = 0; r < 16; ++r) v[r] = acc[t][mt][r];
                stage_tile(v, row, 320 * wn + 64 * t);
            }
        }
    }
    }
    __syncthreads();
    {
        const LAS char* stg = lds + G1_SLOT;
        constexpr int CPR = FFN ? 16 : 40, NP = FFN ? 4 : 10;
#pragma unroll
        for (int e = 0; e < NP; ++e) {
            const int ch = (w * NP + e) * 64 + l, row = ch / CPR, cp = ch % CPR;
            const u32x4 v = *(const LAS u32x4*)(stg + row * EP + cp * 16);
            if (c0 + 8 * cp <= cmax) *(u32x4*)(out + (size_t)(rb * 128 + row) * ldo + c0 + 8 * cp) = v;
        }
    }
}

__global__ __launch_bounds__(512, 2) void k_gemm1(const u16* __restrict__ xn, const u16* __restrict__ wqkv, const u16* __restrict__ w1, const u16* __restrict__ w3,
                                                  u16* __restrict__ qkv, u16* __restrict__ h) {
    __shared__ __attribute__((aligned(16))) char smem[G1_S * G1_SLOT];
    const int b = blockIdx.x, xcd = b & 7, j = b >> 3;
    const int rb = 8 * (xcd & 1) + (j & 7), cg = (xcd >> 1) + 4 * (j >> 3);
    if (cg < 5) gemm1_body<false>(smem, xn, wqkv, wqkv, NQKV, cg * 320, NQKV - 8, qkv, NQKV, rb);
    else gemm1_body<true>(smem, xn, w1, w3, F, (cg - 5) * 128, F - 8, h, F, rb);
}

constexpr int AT_KOFF = 0, AT_VOFF = 40960, AT_ONES = 81920, AD_RING = 82944, AD_SLOT = 16384, AD_S = 4, AD_N = 22, AD_LDS = AD_RING + AD_S * AD_SLOT;
__global__ __launch_bounds__(1024) void k_attn_down(const u16* __restrict__ qkv, u16* __restrict__ o, const u16* __restrict__ h, const u16* __restrict__ w2, u16* __restrict__ gout) {
    __shared__ __attribute__((aligned(16))) char smem[AD_LDS];
    const int b = blockIdx.x, xcd = b & 7, j = b >> 3;
    const int tid = threadIdx.x, l = tid & 63, w = __builtin_amdgcn_readfirstlane(tid >> 6);
    const unsigned lds0 = (unsigned)(uintptr_t)smem;
    const LAS char* lds = (const LAS char*)smem;
    if (w >= 12) {
        const int lw = w - 12, row0 = (4 * xcd + (j & 3)) * 64, col0 = (j >> 2) * 64;
        unsigned toh[2], wof[2], wofl[2];
#pragma unroll
        for (int e = 0; e < 2; ++e) {
            const int pc = 2 * lw + e, r = 8 * pc + (l >> 3);
            toh[e] = (unsigned)((row0 + r) * F + 8 * ((l & 7) ^ ((r >> 1) & 7))) * 2u;
            const int cw = (l & 7) ^ (((r >> 1) & 1) << 2);
            wof[e] = (unsigned)(r * D + col0 + 8 * cw) * 2u;
            const int krl = r + 1344 > F - 1 ? F - 1 : r + 1344;
            wofl[e] = (unsigned)(krl * D + col0 + 8 * cw) * 2u;
        }
        auto issue = [&](int st) {
            const unsigned dst = lds0 + AD_RING + (unsigned)(st % AD_S) * AD_SLOT + (unsigned)(2 * lw) * 1024u;
#pragma unroll
            for (int e = 0; e < 2; ++e) {
                glds16(h, toh[e] + st * 128, dst + e * 1024);
                glds16(w2, st < AD_N - 1 ? wof[e] + st * 64 * D * 2 : wofl[e], dst + 8192 + e * 1024);
            }
        };
        issue(0); issue(1); issue(2);
        wait_bar<8>();
        issue(3);
        static_for<0, AD_N>([&](auto ic) {
            constexpr int st = decltype(ic)::value;
            wait_bar<4 * (imin(st + AD_S - 1, AD_N - 1) - imin(st + 1, AD_N - 1))>();
            if constexpr (st + AD_S < AD_N) issue(st + AD_S);
        });
        return;
    }
    if (w >= 8) {
        const int cw_ = w - 8, wm = cw_ >> 1, wn = cw_ & 1, row0 = (4 * xcd + (j & 3)) * 64, col0 = (j >> 2) * 64;
        const int c = l & 31, hh = l >> 5, i16 = l & 15, gb = (l >> 4) & 1, q = i16 >> 2, p = l & 3;
        const int trow = 32 * wm + c, tsw = (trow >> 1) & 7;
        const int wa = AD_RING + 8192 + (8 * hh + q) * 128 + ((wn ^ ((q >> 1) & 1)) * 64) + gb * 32 + 8 * p;
        f32x16 accB = {};
        bf16x8 tf[2][4], wf[2][4];
        auto read_frags = [&](int st, int buf) {
            const LAS char* sb = lds + (st % AD_S) * AD_SLOT;
#pragma unroll
            for (int s = 0; s < 4; ++s) {
                tf[buf][s] = lds_b128(sb + AD_RING + trow * 128 + (((2 * s + hh) ^ tsw) * 16));
                wf[buf][s] = cat(tr_read(sb + wa + s * 2048), tr_read(sb + wa + s * 2048 + 512));
            }
        };
        asm volatile("s_waitcnt lgkmcnt(0)\n\ts_barrier" ::: "memory");
        read_frags(0, 0);
        static_for<0, AD_N>([&](auto ic) {
            constexpr int st = decltype(ic)::value, buf = st & 1, NS = st == AD_N - 1 ? 2 : 4;
            asm volatile("s_waitcnt lgkmcnt(0)\n\ts_barrier" ::: "memory");
            __builtin_amdgcn_sched_barrier(0);
            if constexpr (st + 1 < AD_N) read_frags(st + 1, buf ^ 1);
            __builtin_amdgcn_sched_barrier(0);
#pragma unroll
            for (int s = 0; s < NS; ++s) accB = MFMA32(wf[buf][s], tf[buf][s], accB);
            __builtin_amdgcn_sched_barrier(0);
        });
        float v[16];
#pragma unroll
        for (int r = 0; r < 16; ++r) v[r] = silu_f(rbf(accB[r]));
        unsigned a0 = cvt_pk(v[0], v[1]), a1 = cvt_pk(v[2], v[3]), b0 = cvt_pk(v[4], v[5]), b1 = cvt_pk(v[6], v[7]);
        unsigned c0_ = cvt_pk(v[8], v[9]), c1 = cvt_pk(v[10], v[11]), d0 = cvt_pk(v[12], v[13]), d1 = cvt_pk(v[14], v[15]);
        auto r0 = __builtin_amdgcn_permlane32_swap(a0, b0, false, false), r1 = __builtin_amdgcn_permlane32_swap(a1, b1, false, false);
        auto r2 = __builtin_amdgcn_permlane32_swap(c0_, d0, false, false), r3 = __builtin_amdgcn_permlane32_swap(c1, d1, false, false);
        u16* orow = gout + (size_t)(row0 + 32 * wm + c) * D + col0 + 32 * wn + 8 * hh;
        *(u32x4*)(orow) = (u32x4){r0[0], r1[0], r0[1], r1[1]};
        *(u32x4*)(orow + 16) = (u32x4){r2[0], r3[0], r2[1], r3[1]};
        return;
    }
#define ABAR() asm volatile("s_barrier" ::: "memory")
    const int qb = 4 * xcd + (j & 3), hg = j >> 2;
    const int q0 = qb * 64;
    const int hd = w & 3, qs = w >> 2;
    const int c = l & 31, hh = l >> 5;
    bf16x8 qf;
    asm volatile("global_load_dwordx4 %0, %1, %2" : "=v"(qf) : "v"((unsigned)((q0 + 32 * qs + c) * NQKV + (hg * 4 + hd) * 16 + 8 * hh) * 2u), "s"(qkv) : "memory");
#pragma unroll
    for (int kv = 0; kv < 2; ++kv)
#pragma unroll
        for (int e = 0; e < 5; ++e) {
            const int piece = w * 5 + e, row = piece * 8 + (l >> 3), cp = l & 7;
            int key = q0 - 256 + row; key = key < 0 ? 0 : key;
            const int cs = kv == 0 ? cp ^ ((row >> 1) & 7) : cp ^ (((row >> 1) & 3) << 1);
            glds16(qkv, (unsigned)(key * NQKV + 512 * (kv + 1) + hg * 64 + 8 * cs) * 2u, lds0 + (kv ? AT_VOFF : AT_KOFF) + piece * 1024);
        }
    if (tid < 16) *(LAS unsigned*)((LAS char*)smem + AT_ONES + tid * 4) = 0x3f803f80u;
    asm volatile("s_waitcnt vmcnt(5) lgkmcnt(0)\n\ts_barrier" : "+v"(qf) :: "memory");
    const int kt0 = q0 + 32 * qs - 256;
    const int tmin = kt0 >= 0 ? 0 : (-kt0) >> 5;
    unsigned pk[9][8];
    float mx = -INFINITY;
    {
        const LAS char* kaddr = lds + AT_KOFF + (32 * qs + c) * 128 + (((2 * hd + hh) ^ ((c >> 1) & 7)) * 16);
        bf16x8 kf[9];
#pragma unroll
        for (int tt = 0; tt < 9; ++tt) kf[tt] = lds_b128(kaddr + tt * 4096);
        f32x16 sc[2];
        sc[0] = MFMA32(kf[0], qf, (f32x16){});
#pragma unroll
        for (int tt = 0; tt < 9; ++tt) {
            if (tt + 1 < 9) sc[(tt + 1) & 1] = MFMA32(kf[tt + 1], qf, (f32x16){});
            f32x16& v = sc[tt & 1];
            if (tt < tmin) {
#pragma unroll
                for (int r = 0; r < 16; ++r) v[r] = -INFINITY;
            }
            if (tt == 0 || tt == 8) {
#pragma unroll
                for (int r = 0; r < 16; ++r) {
                    const int key = (r & 3) + 8 * (r >> 2) + 4 * hh;
                    const bool ok = tt == 0 ? key > c : key <= c;
                    v[r] = ok ? v[r] : -INFINITY;
                }
            }
#pragma unroll
            for (int r = 0; r < 16; ++r) mx = __builtin_fmaxf(mx, v[r]);
#pragma unroll
            for (int e = 0; e < 8; ++e) { pk[tt][e] = cvt_pk(v[2 * e], v[2 * e + 1]); asm volatile("" : "+v"(pk[tt][e])); }
            ABAR();
        }
    }
    { auto rr = __builtin_amdgcn_permlane32_swap(__float_as_uint(mx), __float_as_uint(mx), false, false); mx = __builtin_fmaxf(__uint_as_float(rr[0]), __uint_as_float(rr[1])); }
    constexpr float CS = 0.25f * LOG2E;
    const float nmc = -rbf(mx) * CS;
    const int i16 = l & 15, q4 = i16 >> 2, p4 = l & 3, onesl = (l >> 4) & 1;
    const unsigned vr = 32 * qs + 4 * hh + q4;
    const unsigned va0 = onesl ? AT_ONES + 8 * p4 : AT_VOFF + vr * 128 + (((2 * hd) ^ (((vr >> 1) & 3) << 1)) * 16) + 8 * p4, vstep = onesl ? 0u : 1024u;
    asm volatile("s_waitcnt vmcnt(0)\n\ts_barrier" ::: "memory");
    f32x16 oa = {};
    bf16x8 vf[2][2];
    auto vread = [&](int tt, int buf) {
#pragma unroll
        for (int s = 0; s < 2; ++s) vf[buf][s] = cat(tr_read(lds + (va0 + vstep * (4 * tt + 2 * s))), tr_read(lds + (va0 + vstep * (4 * tt + 2 * s + 1))));
    };
    vread(0, 0);
    const f32x2 cs2 = {CS, CS}, nm2 = {nmc, nmc};
#pragma unroll
    for (int tt = 0; tt < 9; ++tt) {
        __builtin_amdgcn_sched_barrier(0);
        if (tt + 1 < 9) vread(tt + 1, (tt + 1) & 1);
        __builtin_amdgcn_sched_barrier(0);
        unsigned pp[8];
        f32x2 xv[8];
#pragma unroll
        for (int e = 0; e < 8; ++e) xv[e] = (f32x2){bflo(pk[tt][e]), bfhi(pk[tt][e])};
        __builtin_amdgcn_sched_barrier(0);
#pragma unroll
        for (int e = 0; e < 8; ++e) xv[e] = __builtin_elementwise_fma(xv[e], cs2, nm2);
        __builtin_amdgcn_sched_barrier(0);
#pragma unroll
        for (int e = 0; e < 8; ++e) { xv[e].x = __builtin_amdgcn_exp2f(xv[e].x); xv[e].y = __builtin_amdgcn_exp2f(xv[e].y); }
        __builtin_amdgcn_sched_barrier(0);
#pragma unroll
        for (int e = 0; e < 8; ++e) pp[e] = cvt_pk(xv[e].x, xv[e].y);
        __builtin_amdgcn_sched_barrier(0);
#pragma unroll
        for (int s = 0; s < 2; ++s) {
            const bf16x8 pf = __builtin_bit_cast(bf16x8, (u32x4){pp[4 * s], pp[4 * s + 1], pp[4 * s + 2], pp[4 * s + 3]});
            oa = MFMA32(vf[tt & 1][s], pf, oa);
        }
        ABAR();
    }
    ABAR(); ABAR(); ABAR();
#undef ABAR
    const float rl = 1.0f / oa[8];
    unsigned a0 = cvt_pk(oa[0] * rl, oa[1] * rl), a1 = cvt_pk(oa[2] * rl, oa[3] * rl);
    unsigned b0 = cvt_pk(oa[4] * rl, oa[5] * rl), b1 = cvt_pk(oa[6] * rl, oa[7] * rl);
    auto r0 = __builtin_amdgcn_permlane32_swap(a0, b0, false, false);
    auto r1 = __builtin_amdgcn_permlane32_swap(a1, b1, false, false);
    *(u32x4*)(o + (size_t)(q0 + 32 * qs + c) * D + (hg * 4 + hd) * 16 + 8 * hh) = (u32x4){r0[0], r1[0], r0[1], r1[1]};
}

constexpr int G2_SLOT = 32768, G2_N = 4;
__global__ __launch_bounds__(512, 2) void k_final(const u16* __restrict__ o, const u16* __restrict__ wo, const u16* __restrict__ gin, const u16* __restrict__ x, u16* __restrict__ out) {
    __shared__ __attribute__((aligned(16))) char smem[G2_N * G2_SLOT];
    const int b = blockIdx.x, xcd = b & 7, j = b >> 3;
    const int rt = 4 * xcd + (j & 3), ct = j >> 2;
    const int row0 = rt * 64, col0 = ct * 64;
    const int tid = threadIdx.x, l = tid & 63, w = __builtin_amdgcn_readfirstlane(tid >> 6);
    const unsigned lds0 = (unsigned)(uintptr_t)smem;
    const LAS char* lds = (const LAS char*)smem;
    if (w >= 4) {
        const int lw = w - 4;
#pragma unroll
        for (int st = 0; st < G2_N; ++st)
#pragma unroll
            for (int e = 0; e < 4; ++e) {
                const int pc = 4 * lw + e;
                const int r = 4 * pc + (l >> 4), ctk = (l & 15) ^ (r & 15);
                glds16(o, (unsigned)((row0 + r) * D + st * 128 + 8 * ctk) * 2u, lds0 + st * G2_SLOT + pc * 1024);
                const int kr = 8 * pc + (l >> 3), cw = (l & 7) ^ (((kr >> 1) & 1) << 2);
                glds16(wo, (unsigned)((st * 128 + kr) * D + col0 + 8 * cw) * 2u, lds0 + st * G2_SLOT + 16384 + pc * 1024);
            }
        wait_bar<24>(); wait_bar<16>(); wait_bar<8>(); wait_bar<0>();
        return;
    }
    const int wm = w >> 1, wn = w & 1;
    const int c = l & 31, hh = l >> 5, i16 = l & 15, gb = (l >> 4) & 1, q = i16 >> 2, p = l & 3;
    const int trow = (32 * wm + c) * 256, tsw = c & 15;
    const int wa = 16384 + (8 * hh + q) * 128 + ((wn ^ ((q >> 1) & 1)) * 64) + gb * 32 + 8 * p;
    const int row = row0 + 32 * wm + c, colb = col0 + 32 * wn;
    u32x2 xv[4], gv[4];
#pragma unroll
    for (int u = 0; u < 4; ++u) { xv[u] = *(const u32x2*)(x + (size_t)row * D + colb + 8 * u + 4 * hh); gv[u] = *(const u32x2*)(gin + (size_t)row * D + colb + 8 * u + 4 * hh); }
    f32x16 accS = {};
#pragma unroll
    for (int st = 0; st < G2_N; ++st) {
        asm volatile("s_barrier" ::: "memory");
        const LAS char* sb = lds + st * G2_SLOT;
        bf16x8 tf[8], wf[8];
#pragma unroll
        for (int s = 0; s < 8; ++s) {
            tf[s] = lds_b128(sb + trow + (((2 * s + hh) ^ tsw) * 16));
            wf[s] = cat(tr_read(sb + wa + s * 2048), tr_read(sb + wa + s * 2048 + 512));
        }
#pragma unroll
        for (int s = 0; s < 8; ++s) accS = MFMA32(wf[s], tf[s], accS);
    }
    float v[16];
#pragma unroll
    for (int u = 0; u < 4; ++u)
#pragma unroll
        for (int e = 0; e < 4; e += 2) {
            const unsigned sp = cvt_pk(accS[4 * u + e], accS[4 * u + e + 1]), gp = gv[u][e >> 1];
            const unsigned mp = cvt_pk(bflo(sp) * bflo(gp), bfhi(sp) * bfhi(gp));
            const unsigned xw = xv[u][e >> 1];
            v[4 * u + e] = bflo(xw) + bflo(mp); v[4 * u + e + 1] = bfhi(xw) + bfhi(mp);
        }
    unsigned a0 = cvt_pk(v[0], v[1]), a1 = cvt_pk(v[2], v[3]), b0 = cvt_pk(v[4], v[5]), b1 = cvt_pk(v[6], v[7]);
    unsigned c0_ = cvt_pk(v[8], v[9]), c1 = cvt_pk(v[10], v[11]), d0 = cvt_pk(v[12], v[13]), d1 = cvt_pk(v[14], v[15]);
    auto r0 = __builtin_amdgcn_permlane32_swap(a0, b0, false, false), r1 = __builtin_amdgcn_permlane32_swap(a1, b1, false, false);
    auto r2 = __builtin_amdgcn_permlane32_swap(c0_, d0, false, false), r3 = __builtin_amdgcn_permlane32_swap(c1, d1, false, false);
    u16* orow = out + (size_t)row * D + colb + 8 * hh;
    *(u32x4*)(orow) = (u32x4){r0[0], r1[0], r0[1], r1[1]};
    *(u32x4*)(orow + 16) = (u32x4){r2[0], r3[0], r2[1], r3[1]};
}

extern "C" void kernel_launch(void* const* d_in, const int* in_sizes, int n_in, void* d_out, int out_size, void* d_ws, size_t ws_size, hipStream_t stream) {
    const u16* x = (const u16*)d_in[0];
    const u16* wqkv = (const u16*)d_in[1];
    const u16* wo = (const u16*)d_in[2];
    const u16* w1 = (const u16*)d_in[4];
    const u16* w3 = (const u16*)d_in[5];
    const u16* w2 = (const u16*)d_in[6];
    char* ws = (char*)d_ws;
    u16* xn = (u16*)(ws);
    u16* qkv = (u16*)(ws + (4 << 20));
    u16* h = (u16*)(ws + (12 << 20));
    u16* o = (u16*)(ws + (20 << 20));
    u16* g = (u16*)(ws + (24 << 20));
    k_ln<<<S / 4, 256, 0, stream>>>(x, xn, wqkv, w1, w3, wo, w2);
    k_gemm1<<<256, 512, 0, stream>>>(xn, wqkv, w1, w3, qkv, h);
    k_attn_down<<<256, 1024, 0, stream>>>(qkv, o, h, w2, g);
    k_final<<<256, 512, 0, stream>>>(o, wo, g, x, (u16*)d_out);
}
```

```cpp
#include <hip/hip_runtime.h>
#include <stdint.h>

typedef unsigned short u16;
typedef __attribute__((ext_vector_type(8))) short bf16x8;
typedef __attribute__((ext_vector_type(4))) short s16x4;
typedef __attribute__((ext_vector_type(4))) float f32x4;
typedef __attribute__((ext_vector_type(2))) float f32x2;
typedef __attribute__((ext_vector_type(16))) float f32x16;
typedef __attribute__((ext_vector_type(4))) unsigned u32x4;
typedef __attribute__((ext_vector_type(2))) unsigned u32x2;
typedef __attribute__((ext_vector_type(2))) __bf16 bf16x2_t;
#define LAS __attribute__((address_space(3)))
#define DI __device__ __forceinline__

constexpr int S = 2048, D = 512, F = 1376, NQKV = 1536;
constexpr float LOG2E = 1.4426950408889634f;

DI float bflo(unsigned p) { return __uint_as_float(p << 16); }
DI float bfhi(unsigned p) { return __uint_as_float(p & 0xffff0000u); }
DI unsigned cvt_pk(float lo, float hi) { f32x2 v = {lo, hi}; bf16x2_t b = __builtin_convertvector(v, bf16x2_t); return __builtin_bit_cast(unsigned, b); }
DI float rbf(float v) { return bflo(cvt_pk(v, v)); }
DI float silu_f(float v) { return v * __builtin_amdgcn_rcpf(1.0f + __builtin_amdgcn_exp2f(-v * LOG2E)); }
DI s16x4 tr_read(const LAS char* p) { return __builtin_bit_cast(s16x4, __builtin_amdgcn_ds_read_tr16_b64_v4i16((LAS s16x4*)p)); }
DI bf16x8 lds_b128(const LAS char* p) { return *(const LAS bf16x8*)p; }
DI bf16x8 cat(s16x4 lo, s16x4 hi) { return __builtin_shufflevector(lo, hi, 0, 1, 2, 3, 4, 5, 6, 7); }
DI void glds16(const void* sbase, unsigned voff, unsigned lds_dst) {
    unsigned keep;
    asm volatile("s_mov_b32 %0, m0\n\ts_mov_b32 m0, %3\n\ts_nop 0\n\tglobal_load_lds_dwordx4 %1, %2\n\ts_mov_b32 m0, %0" : "=&s"(keep) : "v"(voff), "s"(sbase), "s"(lds_dst) : "memory");
}
template <int N> DI void wait_bar() { asm volatile("s_waitcnt vmcnt(%0) lgkmcnt(0)\n\ts_barrier" ::"i"(N) : "memory"); }
#define MFMA16(a, b, c) __builtin_amdgcn_mfma_f32_16x16x32_bf16((a), (b), (c), 0, 0, 0)
#define MFMA32(a, b, c) __builtin_amdgcn_mfma_f32_32x32x16_bf16((a), (b), (c), 0, 0, 0)
template <int V> struct IC { static constexpr int value = V; };
template <int I, int N, class Fn> DI void static_for(Fn&& f) { if constexpr (I < N) { f(IC<I>{}); static_for<I + 1, N>(f); } }
constexpr int imin(int a, int b) { return a < b ? a : b; }

DI float wave_sum(float v) {
    v += __builtin_bit_cast(float, __builtin_amdgcn_update_dpp(0, __builtin_bit_cast(int, v), 0x111, 0xf, 0xf, true));
    v += __builtin_bit_cast(float, __builtin_amdgcn_update_dpp(0, __builtin_bit_cast(int, v), 0x112, 0xf, 0xf, true));
    v += __builtin_bit_cast(float, __builtin_amdgcn_update_dpp(0, __builtin_bit_cast(int, v), 0x114, 0xf, 0xf, true));
    v += __builtin_bit_cast(float, __builtin_amdgcn_update_dpp(0, __builtin_bit_cast(int, v), 0x118, 0xf, 0xf, true));
    v += __builtin_bit_cast(float, __builtin_amdgcn_update_dpp(0, __builtin_bit_cast(int, v), 0x142, 0xa, 0xf, true));
    v += __builtin_bit_cast(float, __builtin_amdgcn_update_dpp(0, __builtin_bit_cast(int, v), 0x143, 0xc, 0xf, true));
    return __builtin_bit_cast(float, __builtin_amdgcn_readlane(__builtin_bit_cast(int, v), 63));
}
__global__ __launch_bounds__(256) void k_ln(const u16* __restrict__ x, u16* __restrict__ xn, const u16* __restrict__ wqkv, const u16* __restrict__ w1, const u16* __restrict__ w3,
                                              const u16* __restrict__ wo, const u16* __restrict__ w2) {
    unsigned pfv = 0;
    {
        const int gw = blockIdx.x * 4 + (threadIdx.x >> 6), ln = threadIdx.x & 63;
        int idx = gw * 25 + ln;
        if (ln < 25 && idx < 49408) {
            const u16* p;
            if (idx < 12288) p = wqkv + idx * 64;
            else if (idx < 23296) p = w1 + (idx - 12288) * 64;
            else if (idx < 34304) p = w3 + (idx - 23296) * 64;
            else if (idx < 38400) p = wo + (idx - 34304) * 64;
            else p = w2 + (idx - 38400) * 64;
            pfv = *(const unsigned*)p;
        }
    }
    const int row = blockIdx.x * 4 + (threadIdx.x >> 6), lane = threadIdx.x & 63;
    const u32x4 raw = *(const u32x4*)(x + (size_t)row * D + lane * 8);
    float v[8];
#pragma unroll
    for (int j = 0; j < 4; ++j) { v[2 * j] = bflo(raw[j]); v[2 * j + 1] = bfhi(raw[j]); }
    float s = 0.f;
#pragma unroll
    for (int j = 0; j < 8; ++j) s += v[j];
    const float mean = wave_sum(s) * (1.f / D);
    float q = 0.f;
#pragma unroll
    for (int j = 0; j < 8; ++j) { const float d = v[j] - mean; q += d * d; }
    q = wave_sum(q);
    const float var = q * (1.f / D);
    const float mb = rbf(mean), vb = rbf(var);
    const float den = rbf(sqrtf(rbf(vb + 1e-5f)));
    u32x4 o;
#pragma unroll
    for (int j = 0; j < 4; ++j) o[j] = cvt_pk(rbf(v[2 * j] - mb) / den, rbf(v[2 * j + 1] - mb) / den);
    *(u32x4*)(xn + (size_t)row * D + lane * 8) = o;
    asm volatile("" :: "v"(pfv));
}

constexpr int G1_S = 5, G1_SLOT = 28672, G1_N = 16;
template <bool FFN>
DI void gemm1_body(char* smem, const u16* __restrict__ xn, const u16* __restrict__ wa, const u16* __restrict__ wb, const int ldw, const int c0, const int cmax,
                   u16* __restrict__ out, const int ldo, const int rb) {
    constexpr int NT = FFN ? 4 : 5;
    constexpr int PITCH = FFN ? 256 : 640;
    constexpr int NWL = FFN ? 4 : 5;
    constexpr int G = 2 + NWL;
    constexpr int EP = FFN ? 272 : 656;
    const int tid = threadIdx.x, l = tid & 63, w = __builtin_amdgcn_readfirstlane(tid >> 6);
    const unsigned lds0 = (unsigned)(uintptr_t)smem;
    const LAS char* lds = (const LAS char*)smem;
    if (w >= 4) {
        const int lw = w - 4;
        unsigned toff[2];
#pragma unroll
        for (int e = 0; e < 2; ++e) {
            const int row = 16 * (2 * lw + e) + (l >> 2);
            toff[e] = (unsigned)((rb * 128 + row) * D + 8 * ((l & 3) ^ ((row >> 2) & 3))) * 2u;
        }
        unsigned woff[NWL];
#pragma unroll
        for (int e = 0; e < NWL; ++e) {
            if (FFN) {
                const int cl = ((lw + 4 * e) & 7) * 64 + l, r = cl >> 4, c = (cl & 15) ^ ((r & 3) << 2);
                int col = c0 + 8 * c; col = col > cmax ? cmax : col;
                woff[e] = (unsigned)(r * ldw + col) * 2u;
            } else {
                const int cl = (lw + 4 * e) * 64 + l, r = cl / 40, c = (cl % 40) ^ (((r >> 1) & 1) << 2);
                int col = c0 + 8 * c; col = col > cmax ? cmax : col;
                woff[e] = (unsigned)(r * ldw + col) * 2u;
            }
        }
        auto issue = [&](int st) {
            const unsigned dst = lds0 + (unsigned)(st % G1_S) * G1_SLOT;
#pragma unroll
            for (int e = 0; e < 2; ++e) glds16(xn, toff[e] + st * 64, dst + (2 * lw + e) * 1024);
#pragma unroll
            for (int e = 0; e < NWL; ++e) glds16(FFN && e >= 2 ? wb : wa, woff[e] + (unsigned)(st * 32 * ldw) * 2u, dst + 8192 + (lw + 4 * e) * 1024);
        };
        issue(0); issue(1); issue(2); issue(3);
        static_for<0, G1_N>([&](auto ic) {
            constexpr int st = decltype(ic)::value;
            wait_bar<G * (imin(st - 2 + G1_S, G1_N - 1) - imin(st + 1, G1_N - 1))>();
            if constexpr (st - 1 + G1_S < G1_N) issue(st - 1 + G1_S);
        });
    } else {
    const int wm = w >> 1, wn = w & 1;
    const int c = l & 31, hh = l >> 5, i16 = l & 15, gb = (l >> 4) & 1, q = i16 >> 2, p = l & 3;
    int ta[2][2];
#pragma unroll
    for (int mt = 0; mt < 2; ++mt) { ta[mt][0] = (64 * wm + 32 * mt + c) * 64 + ((hh ^ ((c >> 2) & 3)) * 16); ta[mt][1] = ta[mt][0] ^ 32; }
    int ra[4];
    {
        const int base = 8192 + (8 * hh + q) * PITCH + gb * 32 + 8 * p;
        if (FFN) {
#pragma unroll
            for (int t = 0; t < 4; ++t) ra[t] = base + ((t ^ q) * 64);
        } else { const int b = (q >> 1) & 1; ra[0] = base + b * 64; ra[1] = base - b * 64; }
    }
    f32x16 acc[NT][2];
#pragma unroll
    for (int t = 0; t < NT; ++t) { acc[t][0] = (f32x16){}; acc[t][1] = (f32x16){}; }
    bf16x8 tf[2][2], wf[2][NT];
    auto read_frags = [&](int st, int s, int buf) {
        const LAS char* sb = lds + (st % G1_S) * G1_SLOT;
        tf[buf][0] = lds_b128(sb + ta[0][s]); tf[buf][1] = lds_b128(sb + ta[1][s]);
#pragma unroll
        for (int t = 0; t < NT; ++t) {
            const int T = FFN ? 2 * wn + (t & 1) : 5 * wn + t;
            const LAS char* a = FFN ? sb + ra[T & 3] + (t >> 1) * 8192 : sb + ra[T & 1] + T * 64;
            wf[buf][t] = cat(tr_read(a + s * 16 * PITCH), tr_read(a + s * 16 * PITCH + 4 * PITCH));
        }
    };
    auto mma = [&](int buf) {
#pragma unroll
        for (int t = 0; t < NT; ++t) { acc[t][0] = MFMA32(wf[buf][t], tf[buf][0], acc[t][0]); acc[t][1] = MFMA32(wf[buf][t], tf[buf][1], acc[t][1]); }
    };
    auto sched = [&]() {
#pragma unroll
        for (int t = 0; t < NT + 1; ++t) { __builtin_amdgcn_sched_group_barrier(0x100, 2, 0); __builtin_amdgcn_sched_group_barrier(0x008, 1, 0); }
        __builtin_amdgcn_sched_group_barrier(0x008, NT, 0);
    };
    static_for<0, G1_N>([&](auto ic) {
        constexpr int st = decltype(ic)::value;
        asm volatile("s_waitcnt lgkmcnt(0)\n\ts_barrier" ::: "memory");
        if constexpr (st == 0) read_frags(0, 0, 0);
        __builtin_amdgcn_sched_barrier(0);
        read_frags(st, 1, 1);
        mma(0);
        sched();
        __builtin_amdgcn_sched_barrier(0);
        if constexpr (st + 1 < G1_N) { read_frags(st + 1, 0, 0); mma(1); sched(); } else mma(1);
        __builtin_amdgcn_sched_barrier(0);
    });
    LAS char* stg = (LAS char*)smem + G1_SLOT;
    auto stage_tile = [&](const float (&v)[16], int row, int colb) {
        unsigned a0 = cvt_pk(v[0], v[1]), a1 = cvt_pk(v[2], v[3]), b0 = cvt_pk(v[4], v[5]), b1 = cvt_pk(v[6], v[7]);
        unsigned c0_ = cvt_pk(v[8], v[9]), c1 = cvt_pk(v[10], v[11]), d0 = cvt_pk(v[12], v[13]), d1 = cvt_pk(v[14], v[15]);
        auto r0 = __builtin_amdgcn_permlane32_swap(a0, b0, false, false), r1 = __builtin_amdgcn_permlane32_swap(a1, b1, false, false);
        auto r2 = __builtin_amdgcn_permlane32_swap(c0_, d0, false, false), r3 = __builtin_amdgcn_permlane32_swap(c1, d1, false, false);
        LAS char* p_ = stg + row * EP + colb + 16 * hh;
        *(LAS u32x4*)(p_) = (u32x4){r0[0], r1[0], r0[1], r1[1]};
        *(LAS u32x4*)(p_ + 32) = (u32x4){r2[0], r3[0], r2[1], r3[1]};
    };
#pragma unroll
    for (int mt = 0; mt < 2; ++mt) {
        const int row = 64 * wm + 32 * mt + c;
        if (FFN) {
#pragma unroll
            for (int t = 0; t < 2; ++t) {
                float v[16];
#pragma unroll
                for (int r = 0; r < 16; ++r) v[r] = silu_f(acc[t][mt][r]) * acc[t + 2][mt][r];
                stage_tile(v, row, 128 * wn + 64 * t);
            }
        } else {
#pragma unroll
            for (int t = 0; t < 5; ++t) {
                float v[16];
#pragma unroll
                for (int r = 0; r < 16; ++r) v[r] = acc[t][mt][r];
                stage_tile(v, row, 320 * wn + 64 * t);
            }
        }
    }
    }
    __syncthreads();
    {
        const LAS char* stg = lds + G1_SLOT;
        constexpr int CPR = FFN ? 16 : 40, NP = FFN ? 4 : 10;
#pragma unroll
        for (int e = 0; e < NP; ++e) {
            const int ch = (w * NP + e) * 64 + l, row = ch / CPR, cp = ch % CPR;
            const u32x4 v = *(const LAS u32x4*)(stg + row * EP + cp * 16);
            if (c0 + 8 * cp <= cmax) *(u32x4*)(out + (size_t)(rb * 128 + row) * ldo + c0 + 8 * cp) = v;
        }
    }
}

__global__ __launch_bounds__(512, 2) void k_gemm1(const u16* __restrict__ xn, const u16* __restrict__ wqkv, const u16* __restrict__ w1, const u16* __restrict__ w3,
                                                  u16* __restrict__ qkv, u16* __restrict__ h) {
    __shared__ __attribute__((aligned(16))) char smem[G1_S * G1_SLOT];
    const int b = blockIdx.x, xcd = b & 7, j = b >> 3;
    const int rb = 8 * (xcd & 1) + (j & 7), cg = (xcd >> 1) + 4 * (j >> 3);
    if (cg < 5) gemm1_body<false>(smem, xn, wqkv, wqkv, NQKV, cg * 320, NQKV - 8, qkv, NQKV, rb);
    else gemm1_body<true>(smem, xn, w1, w3, F, (cg - 5) * 128, F - 8, h, F, rb);
}

constexpr int AT_KOFF = 0, AT_VOFF = 40960, AT_ONES = 81920, AD_RING = 82944, AD_SLOT = 16384, AD_S = 4, AD_N = 22, AD_LDS = AD_RING + AD_S * AD_SLOT;
__global__ __launch_bounds__(1024) void k_attn_down(const u16* __restrict__ qkv, u16* __restrict__ o, const u16* __restrict__ h, const u16* __restrict__ w2, u16* __restrict__ gout) {
    __shared__ __attribute__((aligned(16))) char smem[AD_LDS];
    const int b = blockIdx.x, xcd = b & 7, j = b >> 3;
    const int tid = threadIdx.x, l = tid & 63, w = __builtin_amdgcn_readfirstlane(tid >> 6);
    const unsigned lds0 = (unsigned)(uintptr_t)smem;
    const LAS char* lds = (const LAS char*)smem;
    if (w >= 12) {
        const int lw = w - 12, row0 = (4 * xcd + (j & 3)) * 64, col0 = (j >> 2) * 64;
        unsigned toh[2], wof[2], wofl[2];
#pragma unroll
        for (int e = 0; e < 2; ++e) {
            const int pc = 2 * lw + e, r = 8 * pc + (l >> 3);
            toh[e] = (unsigned)((row0 + r) * F + 8 * ((l & 7) ^ ((r >> 1) & 7))) * 2u;
            const int cw = (l & 7) ^ (((r >> 1) & 1) << 2);
            wof[e] = (unsigned)(r * D + col0 + 8 * cw) * 2u;
            const int krl = r + 1344 > F - 1 ? F - 1 : r + 1344;
            wofl[e] = (unsigned)(krl * D + col0 + 8 * cw) * 2u;
        }
        auto issue = [&](int st) {
            const unsigned dst = lds0 + AD_RING + (unsigned)(st % AD_S) * AD_SLOT + (unsigned)(2 * lw) * 1024u;
#pragma unroll
            for (int e = 0; e < 2; ++e) {
                glds16(h, toh[e] + st * 128, dst + e * 1024);
                glds16(w2, st < AD_N - 1 ? wof[e] + st * 64 * D * 2 : wofl[e], dst + 8192 + e * 1024);
            }
        };
        issue(0); issue(1); issue(2);
        wait_bar<8>();
        issue(3);
        static_for<0, AD_N>([&](auto ic) {
            constexpr int st = decltype(ic)::value;
            wait_bar<4 * (imin(st + AD_S - 1, AD_N - 1) - imin(st + 1, AD_N - 1))>();
            if constexpr (st + AD_S < AD_N) issue(st + AD_S);
        });
        return;
    }
    if (w >= 8) {
        const int cw_ = w - 8, wm = cw_ >> 1, wn = cw_ & 1, row0 = (4 * xcd + (j & 3)) * 64, col0 = (j >> 2) * 64;
        const int c = l & 31, hh = l >> 5, i16 = l & 15, gb = (l >> 4) & 1, q = i16 >> 2, p = l & 3;
        const int trow = 32 * wm + c, tsw = (trow >> 1) & 7;
        const int wa = AD_RING + 8192 + (8 * hh + q) * 128 + ((wn ^ ((q >> 1) & 1)) * 64) + gb * 32 + 8 * p;
        f32x16 accB = {};
        bf16x8 tf[2][4], wf[2][4];
        auto read_frags = [&](int st, int buf) {
            const LAS char* sb = lds + (st % AD_S) * AD_SLOT;
#pragma unroll
            for (int s = 0; s < 4; ++s) {
                tf[buf][s] = lds_b128(sb + AD_RING + trow * 128 + (((2 * s + hh) ^ tsw) * 16));
                wf[buf][s] = cat(tr_read(sb + wa + s * 2048), tr_read(sb + wa + s * 2048 + 512));
            }
        };
        asm volatile("s_waitcnt lgkmcnt(0)\n\ts_barrier" ::: "memory");
        read_frags(0, 0);
        static_for<0, AD_N>([&](auto ic) {
            constexpr int st = decltype(ic)::value, buf = st & 1, NS = st == AD_N - 1 ? 2 : 4;
            asm volatile("s_waitcnt lgkmcnt(0)\n\ts_barrier" ::: "memory");
            __builtin_amdgcn_sched_barrier(0);
            if constexpr (st + 1 < AD_N) read_frags(st + 1, buf ^ 1);
            __builtin_amdgcn_sched_barrier(0);
#pragma unroll
            for (int s = 0; s < NS; ++s) accB = MFMA32(wf[buf][s], tf[buf][s], accB);
            __builtin_amdgcn_sched_barrier(0);
        });
        float v[16];
#pragma unroll
        for (int r = 0; r < 16; ++r) v[r] = silu_f(rbf(accB[r]));
        unsigned a0 = cvt_pk(v[0], v[1]), a1 = cvt_pk(v[2], v[3]), b0 = cvt_pk(v[4], v[5]), b1 = cvt_pk(v[6], v[7]);
        unsigned c0_ = cvt_pk(v[8], v[9]), c1 = cvt_pk(v[10], v[11]), d0 = cvt_pk(v[12], v[13]), d1 = cvt_pk(v[14], v[15]);
        auto r0 = __builtin_amdgcn_permlane32_swap(a0, b0, false, false), r1 = __builtin_amdgcn_permlane32_swap(a1, b1, false, false);
        auto r2 = __builtin_amdgcn_permlane32_swap(c0_, d0, false, false), r3 = __builtin_amdgcn_permlane32_swap(c1, d1, false, false);
        u16* orow = gout + (size_t)(row0 + 32 * wm + c) * D + col0 + 32 * wn + 8 * hh;
        *(u32x4*)(orow) = (u32x4){r0[0], r1[0], r0[1], r1[1]};
        *(u32x4*)(orow + 16) = (u32x4){r2[0], r3[0], r2[1], r3[1]};
        return;
    }
#define ABAR() asm volatile("s_barrier" ::: "memory")
    const int qb = 4 * xcd + (j & 3), hg = j >> 2;
    const int q0 = qb * 64;
    const int hd = w & 3, qs = w >> 2;
    const int c = l & 31, hh = l >> 5;
    bf16x8 qf;
    asm volatile("global_load_dwordx4 %0, %1, %2" : "=v"(qf) : "v"((unsigned)((q0 + 32 * qs + c) * NQKV + (hg * 4 + hd) * 16 + 8 * hh) * 2u), "s"(qkv) : "memory");
#pragma unroll
    for (int kv = 0; kv < 2; ++kv)
#pragma unroll
        for (int e = 0; e < 5; ++e) {
            const int piece = w * 5 + e, row = piece * 8 + (l >> 3), cp = l & 7;
            int key = q0 - 256 + row; key = key < 0 ? 0 : key;
            const int cs = kv == 0 ? cp ^ ((row >> 1) & 7) : cp ^ (((row >> 1) & 3) << 1);
            glds16(qkv, (unsigned)(key * NQKV + 512 * (kv + 1) + hg * 64 + 8 * cs) * 2u, lds0 + (kv ? AT_VOFF : AT_KOFF) + piece * 1024);
        }
    if (tid < 16) *(LAS unsigned*)((LAS char*)smem + AT_ONES + tid * 4) = 0x3f803f80u;
    asm volatile("s_waitcnt vmcnt(5) lgkmcnt(0)\n\ts_barrier" : "+v"(qf) :: "memory");
    const int kt0 = q0 + 32 * qs - 256;
    const int tmin = kt0 >= 0 ? 0 : (-kt0) >> 5;
    unsigned pk[9][8];
    float mx = -INFINITY;
    {
        const LAS char* kaddr = lds + AT_KOFF + (32 * qs + c) * 128 + (((2 * hd + hh) ^ ((c >> 1) & 7)) * 16);
        bf16x8 kf[9];
#pragma unroll
        for (int tt = 0; tt < 9; ++tt) kf[tt] = lds_b128(kaddr + tt * 4096);
        f32x16 sc[2];
        sc[0] = MFMA32(kf[0], qf, (f32x16){});
#pragma unroll
        for (int tt = 0; tt < 9; ++tt) {
            if (tt + 1 < 9) sc[(tt + 1) & 1] = MFMA32(kf[tt + 1], qf, (f32x16){});
            f32x16& v = sc[tt & 1];
            if (tt < tmin) {
#pragma unroll
                for (int r = 0; r < 16; ++r) v[r] = -INFINITY;
            }
            if (tt == 0 || tt == 8) {
#pragma unroll
                for (int r = 0; r < 16; ++r) {
                    const int key = (r & 3) + 8 * (r >> 2) + 4 * hh;
                    const bool ok = tt == 0 ? key > c : key <= c;
                    v[r] = ok ? v[r] : -INFINITY;
                }
            }
#pragma unroll
            for (int r = 0; r < 16; ++r) mx = __builtin_fmaxf(mx, v[r]);
#pragma unroll
            for (int e = 0; e < 8; ++e) { pk[tt][e] = cvt_pk(v[2 * e], v[2 * e + 1]); asm volatile("" : "+v"(pk[tt][e])); }
            ABAR();
        }
    }
    { auto rr = __builtin_amdgcn_permlane32_swap(__float_as_uint(mx), __float_as_uint(mx), false, false); mx = __builtin_fmaxf(__uint_as_float(rr[0]), __uint_as_float(rr[1])); }
    constexpr float CS = 0.25f * LOG2E;
    const float nmc = -rbf(mx) * CS;
    const int i16 = l & 15, q4 = i16 >> 2, p4 = l & 3, onesl = (l >> 4) & 1;
    const unsigned vr = 32 * qs + 4 * hh + q4;
    const unsigned va0 = onesl ? AT_ONES + 8 * p4 : AT_VOFF + vr * 128 + (((2 * hd) ^ (((vr >> 1) & 3) << 1)) * 16) + 8 * p4, vstep = onesl ? 0u : 1024u;
    asm volatile("s_waitcnt vmcnt(0)\n\ts_barrier" ::: "memory");
    f32x16 oa = {};
    bf16x8 vf[2][2];
    auto vread = [&](int tt, int buf) {
#pragma unroll
        for (int s = 0; s < 2; ++s) vf[buf][s] = cat(tr_read(lds + (va0 + vstep * (4 * tt + 2 * s))), tr_read(lds + (va0 + vstep * (4 * tt + 2 * s + 1))));
    };
    vread(0, 0);
    const f32x2 cs2 = {CS, CS}, nm2 = {nmc, nmc};
#pragma unroll
    for (int tt = 0; tt < 9; ++tt) {
        __builtin_amdgcn_sched_barrier(0);
        if (tt + 1 < 9) vread(tt + 1, (tt + 1) & 1);
        __builtin_amdgcn_sched_barrier(0);
        unsigned pp[8];
        f32x2 xv[8];
#pragma unroll
        for (int e = 0; e < 8; ++e) xv[e] = (f32x2){bflo(pk[tt][e]), bfhi(pk[tt][e])};
        __builtin_amdgcn_sched_barrier(0);
#pragma unroll
        for (int e = 0; e < 8; ++e) xv[e] = __builtin_elementwise_fma(xv[e], cs2, nm2);
        __builtin_amdgcn_sched_barrier(0);
#pragma unroll
        for (int e = 0; e < 8; ++e) { xv[e].x = __builtin_amdgcn_exp2f(xv[e].x); xv[e].y = __builtin_amdgcn_exp2f(xv[e].y); }
        __builtin_amdgcn_sched_barrier(0);
#pragma unroll
        for (int e = 0; e < 8; ++e) pp[e] = cvt_pk(xv[e].x, xv[e].y);
        __builtin_amdgcn_sched_barrier(0);
#pragma unroll
        for (int s = 0; s < 2; ++s) {
            const bf16x8 pf = __builtin_bit_cast(bf16x8, (u32x4){pp[4 * s], pp[4 * s + 1], pp[4 * s + 2], pp[4 * s + 3]});
            oa = MFMA32(vf[tt & 1][s], pf, oa);
        }
        ABAR();
    }
    ABAR(); ABAR(); ABAR();
#undef ABAR
    const float rl = 1.0f / oa[8];
    unsigned a0 = cvt_pk(oa[0] * rl, oa[1] * rl), a1 = cvt_pk(oa[2] * rl, oa[3] * rl);
    unsigned b0 = cvt_pk(oa[4] * rl, oa[5] * rl), b1 = cvt_pk(oa[6] * rl, oa[7] * rl);
    auto r0 = __builtin_amdgcn_permlane32_swap(a0, b0, false, false);
    auto r1 = __builtin_amdgcn_permlane32_swap(a1, b1, false, false);
    *(u32x4*)(o + (size_t)(q0 + 32 * qs + c) * D + (hg * 4 + hd) * 16 + 8 * hh) = (u32x4){r0[0], r1[0], r0[1], r1[1]};
}

constexpr int G2_SLOT = 32768, G2_N = 4;
__global__ __launch_bounds__(512, 2) void k_final(const u16* __restrict__ o, const u16* __restrict__ wo, const u16* __restrict__ gin, const u16* __restrict__ x, u16* __restrict__ out) {
    __shared__ __attribute__((aligned(16))) char smem[G2_N * G2_SLOT];
    const int b = blockIdx.x, xcd = b & 7, j = b >> 3;
    const int rt = 4 * xcd + (j & 3), ct = j >> 2;
    const int row0 = rt * 64, col0 = ct * 64;
    const int tid = threadIdx.x, l = tid & 63, w = __builtin_amdgcn_readfirstlane(tid >> 6);
    const unsigned lds0 = (unsigned)(uintptr_t)smem;
    const LAS char* lds = (const LAS char*)smem;
    if (w >= 4) {
        const int lw = w - 4;
#pragma unroll
        for (int st = 0; st < G2_N; ++st)
#pragma unroll
            for (int e = 0; e < 4; ++e) {
                const int pc = 4 * lw + e;
                const int r = 4 * pc + (l >> 4), ctk = (l & 15) ^ (r & 15);
                glds16(o, (unsigned)((row0 + r) * D + st * 128 + 8 * ctk) * 2u, lds0 + st * G2_SLOT + pc * 1024);
                const int kr = 8 * pc + (l >> 3), cw = (l & 7) ^ (((kr >> 1) & 1) << 2);
                glds16(wo, (unsigned)((st * 128 + kr) * D + col0 + 8 * cw) * 2u, lds0 + st * G2_SLOT + 16384 + pc * 1024);
            }
        wait_bar<24>(); wait_bar<16>(); wait_bar<8>(); wait_bar<0>();
        return;
    }
    const int wm = w >> 1, wn = w & 1;
    const int c = l & 31, hh = l >> 5, i16 = l & 15, gb = (l >> 4) & 1, q = i16 >> 2, p = l & 3;
    const int trow = (32 * wm + c) * 256, tsw = c & 15;
    const int wa = 16384 + (8 * hh + q) * 128 + ((wn ^ ((q >> 1) & 1)) * 64) + gb * 32 + 8 * p;
    const int row = row0 + 32 * wm + c, colb = col0 + 32 * wn;
    u32x2 xv[4], gv[4];
#pragma unroll
    for (int u = 0; u < 4; ++u) { xv[u] = *(const u32x2*)(x + (size_t)row * D + colb + 8 * u + 4 * hh); gv[u] = *(const u32x2*)(gin + (size_t)row * D + colb + 8 * u + 4 * hh); }
    f32x16 accS = {};
#pragma unroll
    for (int st = 0; st < G2_N; ++st) {
        asm volatile("s_barrier" ::: "memory");
        const LAS char* sb = lds + st * G2_SLOT;
        bf16x8 tf[8], wf[8];
#pragma unroll
        for (int s = 0; s < 8; ++s) {
            tf[s] = lds_b128(sb + trow + (((2 * s + hh) ^ tsw) * 16));
            wf[s] = cat(tr_read(sb + wa + s * 2048), tr_read(sb + wa + s * 2048 + 512));
        }
#pragma unroll
        for (int s = 0; s < 8; ++s) accS = MFMA32(wf[s], tf[s], accS);
    }
    float v[16];
#pragma unroll
    for (int u = 0; u < 4; ++u)
#pragma unroll
        for (int e = 0; e < 4; e += 2) {
            const unsigned sp = cvt_pk(accS[4 * u + e], accS[4 * u + e + 1]), gp = gv[u][e >> 1];
            const unsigned mp = cvt_pk(bflo(sp) * bflo(gp), bfhi(sp) * bfhi(gp));
            const unsigned xw = xv[u][e >> 1];
            v[4 * u + e] = bflo(xw) + bflo(mp); v[4 * u + e + 1] = bfhi(xw) + bfhi(mp);
        }
    unsigned a0 = cvt_pk(v[0], v[1]), a1 = cvt_pk(v[2], v[3]), b0 = cvt_pk(v[4], v[5]), b1 = cvt_pk(v[6], v[7]);
    unsigned c0_ = cvt_pk(v[8], v[9]), c1 = cvt_pk(v[10], v[11]), d0 = cvt_pk(v[12], v[13]), d1 = cvt_pk(v[14], v[15]);
    auto r0 = __builtin_amdgcn_permlane32_swap(a0, b0, false, false), r1 = __builtin_amdgcn_permlane32_swap(a1, b1, false, false);
    auto r2 = __builtin_amdgcn_permlane32_swap(c0_, d0, false, false), r3 = __builtin_amdgcn_permlane32_swap(c1, d1, false, false);
    u16* orow = out + (size_t)row * D + colb + 8 * hh;
    *(u32x4*)(orow) = (u32x4){r0[0], r1[0], r0[1], r1[1]};
    *(u32x4*)(orow + 16) = (u32x4){r2[0], r3[0], r2[1], r3[1]};
}

extern "C" void kernel_launch(void* const* d_in, const int* in_sizes, int n_in, void* d_out, int out_size, void* d_ws, size_t ws_size, hipStream_t stream) {
    const u16* x = (const u16*)d_in[0];
    const u16* wqkv = (const u16*)d_in[1];
    const u16* wo = (const u16*)d_in[2];
    const u16* w1 = (const u16*)d_in[4];
    const u16* w3 = (const u16*)d_in[5];
    const u16* w2 = (const u16*)d_in[6];
    char* ws = (char*)d_ws;
    u16* xn = (u16*)(ws);
    u16* qkv = (u16*)(ws + (4 << 20));
    u16* h = (u16*)(ws + (12 << 20));
    u16* o = (u16*)(ws + (20 << 20));
    u16* g = (u16*)(ws + (24 << 20));
    k_ln<<<S / 4, 256, 0, stream>>>(x, xn, wqkv, w1, w3, wo, w2);
    k_gemm1<<<256, 512, 0, stream>>>(xn, wqkv, w1, w3, qkv, h);
    k_attn_down<<<256, 1024, 0, stream>>>(qkv, o, h, w2, g);
    k_final<<<256, 512, 0, stream>>>(o, wo, g, x, (u16*)d_out);
}
```
